```python
import torch

def _softmax(x, axis=-1):
    return torch.softmax(x, dim=axis)
def _gelu(x):
    return torch.nn.functional.gelu(x, approximate="tanh")
def _silu(x):
    return torch.nn.functional.silu(x)
def _sigmoid(x):
    return torch.sigmoid(x)
def _layernorm(x, axis=-1, eps=1e-5):
    m = x.mean(dim=axis, keepdim=True)
    v = x.var(dim=axis, keepdim=True, unbiased=False)
    return (x - m) / torch.sqrt(v + eps)
def _scatter_add(tgt, idx, src, axis=0):
    return tgt.clone().index_add_(axis, idx, src)
def _segment_sum(src, seg, n):
    out = torch.zeros((n,) + src.shape[1:], dtype=src.dtype)
    return out.index_add_(0, seg, src)
def _pad(x, pw):
    flat = tuple(v for pair in reversed(pw) for v in pair)
    return torch.nn.functional.pad(x, flat)

kernel_name = 'gated_mix__softmax_full_mha__glu_gated_bilinear__prompt'

def setup_inputs():

    _w = (lambda a: (lambda t: t.to(torch.bfloat16) if t.is_floating_point() else t)(torch.as_tensor(a)))
    def _A_setup():
        B, S, D = 1, 2048, 512
        return {
            "x": _w((lambda *s: torch.randn(*s, dtype=torch.bfloat16) * 0.05)(B, S, D)),
            "wqkv": _w((lambda *s: torch.randn(*s, dtype=torch.bfloat16) * 0.8)(D, 3 * D)),
            "wo": _w((lambda *s: torch.randn(*s, dtype=torch.bfloat16) * 0.05)(D, D)),
        }
    _A_in = _A_setup()

    def _B_setup():
        B, S, D, F = 1, 2048, 512, 1792
        return {
            "x": _w((lambda *s: torch.randn(*s, dtype=torch.bfloat16) * 0.05)(B, S, D)),
            "w_gate": _w((lambda *s: torch.randn(*s, dtype=torch.bfloat16) * 1.6)(D, F) * (D ** -0.5)),
            "w_up": _w((lambda *s: torch.randn(*s, dtype=torch.bfloat16) * 1.6)(D, F) * (D ** -0.5)),
            "w_down": _w((lambda *s: torch.randn(*s, dtype=torch.bfloat16) * 1.6)(F, D) * (F ** -0.5)),
        }
    _B_in = _B_setup()

    return {
        "A_x": _A_in["x"],
        "A_wqkv": _A_in["wqkv"],
        "A_wo": _A_in["wo"],
        "B_x": _B_in["x"],
        "B_w_gate": _B_in["w_gate"],
        "B_w_up": _B_in["w_up"],
        "B_w_down": _B_in["w_down"],
        "gate_w": _w((lambda *s: torch.randn(*s, dtype=torch.bfloat16) * 0.05)(512) * 16.0),
    }

def reference(A_x, A_wqkv, A_wo, B_x, B_w_gate, B_w_up, B_w_down, gate_w):
    def _A_ref(x, wqkv, wo):
        B, S, D = x.shape
        H = 32; Dh = D // H
        qkv = (lambda x, *d: x.permute(*d))((x @ wqkv).reshape(B, S, 3, H, Dh), 2, 0, 3, 1, 4)
        q, k, v = qkv[0], qkv[1], qkv[2]
        scale = Dh ** -0.5
        scores = torch.einsum("bhqd,bhkd->bhqk", q, k) * scale
        p = _softmax(scores, axis=-1)
        o = torch.einsum("bhqk,bhkd->bhqd", p, v)
        return (lambda x, *d: x.permute(*d))(o, 0, 2, 1, 3).reshape(B, S, D) @ wo

    def _B_ref(x, w_gate, w_up, w_down):
        g = torch.einsum("bsd,df->bsf", x, w_gate)
        u = torch.einsum("bsd,df->bsf", x, w_up)
        h = g * u
        return torch.einsum("bsf,fd->bsd", h, w_down)

    x = A_x
    g = _sigmoid((x * gate_w).sum(-1, keepdims=True))
    a = _A_ref(x, A_wqkv, A_wo)
    b = _B_ref(x, B_w_gate, B_w_up, B_w_down)
    return g * a + (1 - g) * b
```

```cpp
#include <hip/hip_runtime.h>
#include <hip/hip_bf16.h>
typedef float f32x16 __attribute__((ext_vector_type(16)));
typedef float f32x8 __attribute__((ext_vector_type(8)));
typedef __bf16 bf16x8 __attribute__((ext_vector_type(8)));
typedef __bf16 bf16x16 __attribute__((ext_vector_type(16)));
typedef unsigned u32x4 __attribute__((ext_vector_type(4)));
typedef unsigned short u16;
#define MFMA(a, b, c) __builtin_amdgcn_mfma_f32_32x32x16_bf16(a, b, c, 0, 0, 0)
constexpr int S = 2048, D = 512, F = 1792, NKT = 144;
constexpr size_t WS_Q = 0, WS_K = 2u << 20, WS_V = 4u << 20, WS_A = 6u << 20, WS_G = 16u << 20;
constexpr float QSCALE = 0.25f * 1.4426950408889634f;

__device__ __forceinline__ float bf2f(u16 b) { return __uint_as_float((unsigned)b << 16); }
__device__ __forceinline__ u32x4 pack8(const float* v) {
  bf16x8 r; for (int i = 0; i < 8; ++i) r[i] = (__bf16)v[i]; return __builtin_bit_cast(u32x4, r); }
__device__ __forceinline__ bf16x8 wfrag(const u16* W, int ld, int k0, int hh, int col, bool perm) {
  bf16x8 r;
  for (int j = 0; j < 8; ++j) { int k = perm ? k0 + (j & 3) + 8 * (j >> 2) + 4 * hh : k0 + 8 * hh + j; u16 v = W[(long)k * ld + col]; r[j] = __builtin_bit_cast(__bf16, v); }
  return r; }

__global__ void k_gate(const u16* x, const u16* gw, float* g) {
  int tok = blockIdx.x * 4 + (threadIdx.x >> 6), l = threadIdx.x & 63; float s = 0;
  for (int i = 0; i < 8; ++i) s += bf2f(x[tok * D + l * 8 + i]) * bf2f(gw[l * 8 + i]);
  for (int o = 32; o; o >>= 1) s += __shfl_xor(s, o);
  if (l == 0) g[tok] = 1.f / (1.f + __expf(-s));
}
__global__ __launch_bounds__(64) void k1(const u16* x, const u16* wqkv, const u16* wg, const u16* wu, char* ws) {
  int tt = blockIdx.x, ct = blockIdx.y, l = threadIdx.x, hh = l >> 5, r = l & 31;
  f32x16 acc = {};
  for (int s = 0; s < 32; ++s) {
    bf16x8 xf = *(const bf16x8*)(x + (long)(tt * 32 + r) * D + s * 16 + hh * 8);
    bf16x8 wf;
    if (ct < 48) wf = wfrag(wqkv, 1536, s * 16, hh, ct * 32 + r, false);
    else wf = wfrag(r < 16 ? wg : wu, F, s * 16, hh, (ct - 48) * 16 + (r & 15), false);
    if (ct >= 32 && ct < 48) acc = MFMA(xf, wf, acc); else acc = MFMA(wf, xf, acc);
  }
  float v[16]; for (int i = 0; i < 16; ++i) v[i] = acc[i];
  if (ct < 16) { for (int i = 0; i < 16; ++i) v[i] *= QSCALE;
    for (int j = 0; j < 2; ++j) *(u32x4*)(ws + WS_Q + ((size_t)(ct * 2 + j) * 64 + tt) * 1024 + l * 16) = pack8(v + 8 * j);
  } else if (ct < 32) { int pr = (r & 0x13) | ((r & 4) << 1) | ((r & 8) >> 1);
    for (int j = 0; j < 2; ++j) *(u32x4*)(ws + WS_K + ((size_t)((ct - 16) * 2 + j) * 64 + tt) * 1024 + hh * 512 + pr * 16) = pack8(v + 8 * j);
  } else if (ct < 48) { int head = (ct - 32) * 2 + (r >> 4), d = r & 15;
    for (int s = 0; s < 2; ++s) *(u32x4*)(ws + WS_V + ((size_t)head * 64 + tt) * 1024 + s * 512 + hh * 256 + d * 16) = pack8(v + 8 * s);
  } else { float g = ((const float*)(ws + WS_G))[tt * 32 + r]; float h[8]; for (int i = 0; i < 8; ++i) h[i] = v[i] * v[i + 8] * (1.f - g);
    *(u32x4*)(ws + WS_A + ((size_t)tt * NKT + (ct - 48)) * 1024 + l * 16) = pack8(h);
  }
}
__global__ __launch_bounds__(64) void k2(char* ws) {
  int tt = blockIdx.x, head = blockIdx.y, l = threadIdx.x, hh = l >> 5, r = l & 31;
  bf16x8 q = *(const bf16x8*)(ws + WS_Q + ((size_t)head * 64 + tt) * 1024 + l * 16);
  const char* Kp = ws + WS_K + (size_t)head * 65536 + l * 16; const char* Vp = ws + WS_V + (size_t)head * 65536 + hh * 256 + (r & 15) * 16;
  f32x16 z = {}; f32x16 s0 = MFMA(*(const bf16x8*)Kp, q, z);
  float m = s0[0]; for (int i = 1; i < 16; ++i) m = fmaxf(m, s0[i]); m = fmaxf(m, __shfl_xor(m, 32));
  f32x16 nm; for (int i = 0; i < 16; ++i) nm[i] = -m;
  f32x16 o = {}; u32x4 ones = {0x3f803f80u, 0x3f803f80u, 0x3f803f80u, 0x3f803f80u};
  for (int t = 0; t < 64; ++t) {
    f32x16 s = MFMA(*(const bf16x8*)(Kp + t * 1024), q, nm);
    for (int i = 0; i < 16; ++i) s[i] = __builtin_amdgcn_exp2f(s[i]);
    u32x4 v0 = *(const u32x4*)(Vp + t * 1024), v1 = *(const u32x4*)(Vp + t * 1024 + 512);
    if (r >= 16) { v0 = ones; v1 = ones; }
    f32x8 lo = __builtin_shufflevector(s, s, 0, 1, 2, 3, 4, 5, 6, 7), hi = __builtin_shufflevector(s, s, 8, 9, 10, 11, 12, 13, 14, 15);
    o = __builtin_amdgcn_smfmac_f32_32x32x32_bf16(__builtin_bit_cast(bf16x8, v0), __builtin_bit_cast(bf16x16, lo), o, (int)0xddddddddu, 0, 0);
    o = __builtin_amdgcn_smfmac_f32_32x32x32_bf16(__builtin_bit_cast(bf16x8, v1), __builtin_bit_cast(bf16x16, hi), o, (int)0xddddddddu, 0, 0);
  }
  float g = ((const float*)(ws + WS_G))[tt * 32 + r]; float sc = g / o[8]; float v[8]; for (int i = 0; i < 8; ++i) v[i] = o[i] * sc;
  *(u32x4*)(ws + WS_A + ((size_t)tt * NKT + 112 + head) * 1024 + l * 16) = pack8(v);
}
__global__ __launch_bounds__(64) void k3(const u16* wd, const u16* wo, const char* ws, u16* out) {
  int tt = blockIdx.x, nt = blockIdx.y, l = threadIdx.x, hh = l >> 5, r = l & 31;
  int n = nt * 32 + 16 * ((r >> 2) & 1) + 4 * (r >> 3) + (r & 3);
  f32x16 acc = {};
  for (int kt = 0; kt < NKT; ++kt) {
    bf16x8 af = *(const bf16x8*)(ws + WS_A + ((size_t)tt * NKT + kt) * 1024 + l * 16);
    bf16x8 wf = kt < 112 ? wfrag(wd, D, kt * 16, hh, n, true) : wfrag(wo, D, (kt - 112) * 16, hh, n, true);
    acc = MFMA(wf, af, acc);
  }
  float v[16]; for (int i = 0; i < 16; ++i) v[i] = acc[i];
  u16* o = out + (long)(tt * 32 + r) * D + nt * 32 + 16 * hh;
  *(u32x4*)o = pack8(v); *(u32x4*)(o + 8) = pack8(v + 8);
}
extern "C" void kernel_launch(void* const* d_in, const int* in_sizes, int n_in, void* d_out, int out_size, void* d_ws, size_t ws_size, hipStream_t stream) {
  const u16* x = (const u16*)d_in[0]; const u16* wqkv = (const u16*)d_in[1]; const u16* wo = (const u16*)d_in[2];
  const u16* wg = (const u16*)d_in[4]; const u16* wu = (const u16*)d_in[5]; const u16* wd = (const u16*)d_in[6]; const u16* gw = (const u16*)d_in[7];
  char* ws = (char*)d_ws;
  k_gate<<<S / 4, 256, 0, stream>>>(x, gw, (float*)(ws + WS_G));
  k1<<<dim3(64, 160), 64, 0, stream>>>(x, wqkv, wg, wu, ws);
  k2<<<dim3(64, 32), 64, 0, stream>>>(ws);
  k3<<<dim3(64, 16), 64, 0, stream>>>(wd, wo, ws, (u16*)d_out);
}
```

```cpp
#include <hip/hip_runtime.h>
#include <hip/hip_bf16.h>
__global__ __launch_bounds__(512, 2) void gated_mix__softmax_full_mha__glu_gated_bilinear__prompt_kernel(const void* x, const void* wqkv, const void* wo, const void* wg, const void* wu, const void* wd, const void* gw, void* out, void* ws) {
  __shared__ __attribute__((aligned(16))) char lds[163840];
  asm volatile(
    "L_start_%=:\n\t"
    "s_load_dwordx16 s[12:27], %0, 0x0\n\t"
    "s_load_dwordx2 s[28:29], %0, 0x40\n\t"
    "v_lshrrev_b32 v208, 6, %2\n\t"
    "v_and_b32 v1, 63, %2\n\t"
    "v_readfirstlane_b32 s8, v208\n\t"
    "s_getpc_b64 s[84:85]\n\t"
    "L_pe_%=:\n\t"
    "s_sub_u32 s84, s84, L_pe_%=-L_start_%=\n\t"
    "s_subb_u32 s85, s85, 0\n\t"
    "s_mov_b32 s86, L_end_%=-L_start_%=\n\t"
    "s_mov_b32 s87, 0x20000\n\t"
    "s_and_b32 s11, %1, -8\n\t"
    "s_add_u32 s11, s11, s8\n\t"
    "s_lshl_b32 s11, s11, 7\n\t"
    "v_lshlrev_b32 v215, 15, v1\n\t"
    "s_mov_b64 exec, 3\n\t"
    "buffer_load_dword v254, v215, s[84:87], s11 offen\n\t"
    "s_mov_b64 exec, -1\n\t"
    "s_bfe_u32 s9, %1, 0x20001\n\t"
    "s_lshl_b32 s9, s9, 2\n\t"
    "s_bfe_u32 s30, %1, 0x20003\n\t"
    "s_add_u32 s9, s9, s30\n\t"
    "s_and_b32 s10, %1, 1\n\t"
    "s_lshl_b32 s10, s10, 3\n\t"
    "s_bfe_u32 s11, %1, 0x30005\n\t"
    "s_add_u32 s10, s10, s11\n\t"
    "s_add_u32 s30, s9, s10\n\t"
    "s_and_b32 s74, s30, 7\n\t"
    "s_cmp_lt_u32 s8, 4\n\t"
    "s_cbranch_scc1 L_half0_%=\n\t"
    "s_and_b32 s31, s8, 3\n\t"
    "s_lshl_b32 s11, s9, 4\n\t"
    "s_add_u32 s11, s11, s31\n\t"
    "s_lshl_b32 s11, s11, 13\n\t"
    "s_lshl_b32 s30, s31, 2\n\t"
    "v_lshrrev_b32 v208, 4, v1\n\t"
    "v_add_u32 v208, s30, v208\n\t"
    "v_xor_b32 v208, v208, v1\n\t"
    "v_and_b32 v208, 7, v208\n\t"
    "v_lshrrev_b32 v209, 3, v1\n\t"
    "v_lshlrev_b32 v210, 10, v209\n\t"
    "v_lshl_or_b32 v13, v208, 4, v210\n\t"
    "v_add_u32 v13, s11, v13\n\t"
    "s_lshl_b32 s33, s31, 10\n\t"
    "s_lshl_b32 s34, s31, 11\n\t"
    "s_mul_i32 s11, s31, 0xc000\n\t"
    "s_lshr_b32 s30, s10, 1\n\t"
    "s_lshl_b32 s30, s30, 7\n\t"
    "s_add_u32 s30, s11, s30\n\t"
    "s_and_b32 s31, s10, 1\n\t"
    "s_lshl_b32 s31, s31, 10\n\t"
    "s_add_u32 s30, s30, s31\n\t"
    "v_mul_u32_u24 v209, 0xc00, v209\n\t"
    "v_bfe_u32 v208, v1, 4, 1\n\t"
    "v_lshlrev_b32 v208, 2, v208\n\t"
    "v_xor_b32 v208, v208, v1\n\t"
    "v_and_b32 v208, 7, v208\n\t"
    "v_lshl_add_u32 v14, v208, 4, v209\n\t"
    "v_add_u32 v14, s30, v14\n\t"
    "s_lshl_b32 s31, s10, 6\n\t"
    "s_add_u32 s11, s11, s31\n\t"
    "s_addk_i32 s11, 0x800\n\t"
    "v_lshrrev_b32 v208, 2, v1\n\t"
    "v_mul_u32_u24 v208, 0xc00, v208\n\t"
    "v_and_b32 v209, 3, v1\n\t"
    "v_lshl_add_u32 v17, v209, 4, v208\n\t"
    "v_add_u32 v17, s11, v17\n\t"
    "s_add_u32 s11, s74, 1\n\t"
    "s_and_b32 s75, s11, 7\n\t"
    "s_add_u32 s11, s74, 2\n\t"
    "s_and_b32 s76, s11, 7\n\t"
    "s_waitcnt lgkmcnt(0)\n\t"
    "s_mov_b64 s[36:37], s[12:13]\n\t"
    "s_mov_b32 s38, 0x200000\n\t"
    "s_mov_b32 s39, 0x20000\n\t"
    "s_mov_b64 s[40:41], s[14:15]\n\t"
    "s_mov_b32 s42, 0x180000\n\t"
    "s_mov_b32 s43, 0x20000\n\t"
    "s_add_u32 m0, s33, 0\n\t"
    "s_lshl_b32 s4, s74, 7\n\t"
    "buffer_load_dwordx4 v13, s[36:39], s4 offen lds\n\t"
    "s_add_u32 m0, s33, 0x1000\n\t"
    "s_lshl_b32 s4, s74, 7\n\t"
    "s_add_u32 s4, s4, 0x8000\n\t"
    "buffer_load_dwordx4 v13, s[36:39], s4 offen lds\n\t"
    "s_add_u32 m0, s33, 0x2000\n\t"
    "s_lshl_b32 s4, s74, 7\n\t"
    "s_add_u32 s4, s4, 0x10000\n\t"
    "buffer_load_dwordx4 v13, s[36:39], s4 offen lds\n\t"
    "s_add_u32 s11, s74, 3\n\t"
    "s_and_b32 s77, s11, 7\n\t"
    "s_add_u32 s11, s74, 4\n\t"
    "s_and_b32 s78, s11, 7\n\t"
    "s_add_u32 s11, s74, 5\n\t"
    "s_and_b32 s79, s11, 7\n\t"
    "s_add_u32 s11, s74, 6\n\t"
    "s_add_u32 m0, s33, 0x3000\n\t"
    "s_lshl_b32 s4, s74, 7\n\t"
    "s_add_u32 s4, s4, 0x18000\n\t"
    "buffer_load_dwordx4 v13, s[36:39], s4 offen lds\n\t"
    "s_and_b32 s80, s11, 7\n\t"
    "s_add_u32 s11, s74, 7\n\t"
    "s_and_b32 s81, s11, 7\n\t"
    "s_mov_b64 s[52:53], s[28:29]\n\t"
    "s_mov_b32 s54, 0x1200000\n\t"
    "s_mov_b32 s55, 0x20000\n\t"
    "s_mov_b64 s[44:45], s[18:19]\n\t"
    "s_add_u32 m0, s34, 0x1f000\n\t"
    "s_mul_i32 s4, s74, 0x30000\n\t"
    "buffer_load_dwordx4 v14, s[40:43], s4 offen lds\n\t"
    "s_mov_b32 s46, 0x1c0000\n\t"
    "s_mov_b32 s47, 0x20000\n\t"
    "s_mov_b64 s[48:49], s[20:21]\n\t"
    "s_mov_b32 s50, 0x1c0000\n\t"
    "s_mov_b32 s51, 0x20000\n\t"
    "s_mov_b64 s[68:69], s[24:25]\n\t"
    "s_mov_b32 s70, 0x400\n\t"
    "s_add_u32 m0, s34, 0x1f400\n\t"
    "s_mul_i32 s4, s74, 0x30000\n\t"
    "s_addk_i32 s4, 0x6000\n\t"
    "buffer_load_dwordx4 v14, s[40:43], s4 offen lds\n\t"
    "s_mov_b32 s71, 0x20000\n\t"
    "s_mov_b64 s[56:57], s[22:23]\n\t"
    "s_mov_b32 s58, 0x1c0000\n\t"
    "s_mov_b32 s59, 0x20000\n\t"
    "s_mov_b64 s[60:61], s[16:17]\n\t"
    "s_mov_b32 s62, 0x80000\n\t"
    "s_mov_b32 s63, 0x20000\n\t"
    "s_add_u32 m0, s33, 0x21000\n\t"
    "s_mul_i32 s4, s74, 0x30000\n\t"
    "buffer_load_dwordx4 v17, s[40:43], s4 offen lds\n\t"
    "s_mov_b64 s[64:65], s[26:27]\n\t"
    "s_mov_b32 s66, 0x200000\n\t"
    "s_mov_b32 s67, 0x20000\n\t"
    "v_lshlrev_b32 v203, 4, v1\n\t"
    "s_mov_b32 s11, 0x1005000\n\t"
    "s_buffer_load_dwordx2 s[82:83], s[52:55], s11\n\t"
    "s_mov_b32 s84, 0\n\t"
    "s_add_u32 m0, s33, 0x4000\n\t"
    "s_lshl_b32 s4, s75, 7\n\t"
    "buffer_load_dwordx4 v13, s[36:39], s4 offen lds\n\t"
    "s_and_b32 s12, %1, 7\n\t"
    "s_lshl_b32 s12, s12, 2\n\t"
    "s_bfe_u32 s11, %1, 0x20003\n\t"
    "s_add_u32 s12, s12, s11\n\t"
    "s_bfe_u32 s13, %1, 0x30005\n\t"
    "s_lshr_b32 s14, s10, 3\n\t"
    "s_lshl_b32 s11, s9, 1\n\t"
    "s_add_u32 m0, s33, 0x5000\n\t"
    "s_lshl_b32 s4, s75, 7\n\t"
    "s_add_u32 s4, s4, 0x8000\n\t"
    "buffer_load_dwordx4 v13, s[36:39], s4 offen lds\n\t"
    "s_add_u32 s14, s14, s11\n\t"
    "s_lshl_b32 s11, s13, 3\n\t"
    "s_add_u32 s11, s11, s8\n\t"
    "s_mul_i32 s11, s11, 0x90\n\t"
    "s_add_u32 s11, s11, s12\n\t"
    "s_lshl_b32 s11, s11, 10\n\t"
    "s_add_u32 s11, s11, 0x61c000\n\t"
    "s_add_u32 m0, s33, 0x6000\n\t"
    "s_lshl_b32 s4, s75, 7\n\t"
    "s_add_u32 s4, s4, 0x10000\n\t"
    "buffer_load_dwordx4 v13, s[36:39], s4 offen lds\n\t"
    "v_add_u32 v19, s11, v203\n\t"
    "s_and_b32 s31, s8, 3\n\t"
    "v_mul_u32_u24 v210, 0x124a, v1\n\t"
    "v_lshrrev_b32 v210, 16, v210\n\t"
    "v_mul_u32_u24 v211, 14, v210\n\t"
    "v_sub_u32 v211, v1, v211\n\t"
    "v_mul_u32_u24 v210, 0xe00, v210\n\t"
    "s_add_u32 m0, s33, 0x7000\n\t"
    "s_lshl_b32 s4, s75, 7\n\t"
    "s_add_u32 s4, s4, 0x18000\n\t"
    "buffer_load_dwordx4 v13, s[36:39], s4 offen lds\n\t"
    "v_lshl_add_u32 v15, v211, 4, v210\n\t"
    "s_mul_i32 s11, s8, 0x3800\n\t"
    "s_mul_i32 s30, s10, 0xe0\n\t"
    "s_add_u32 s11, s11, s30\n\t"
    "v_add_u32 v15, s11, v15\n\t"
    "v_add_u32 v22, 0xffff2000, v15\n\t"
    "s_and_b32 s35, s8, 1\n\t"
    "s_add_u32 m0, s34, 0x22000\n\t"
    "s_mul_i32 s4, s75, 0x30000\n\t"
    "buffer_load_dwordx4 v14, s[40:43], s4 offen lds\n\t"
    "s_lshl_b32 s35, s35, 2\n\t"
    "s_lshr_b32 s11, s8, 1\n\t"
    "s_add_u32 s35, s35, s11\n\t"
    "s_mul_i32 s35, s35, 0x380\n\t"
    "s_mov_b32 s72, -1\n\t"
    "s_mov_b32 s73, 0xffffff\n\t"
    "s_lshl_b32 s11, s9, 2\n\t"
    "s_add_u32 m0, s34, 0x22400\n\t"
    "s_mul_i32 s4, s75, 0x30000\n\t"
    "s_addk_i32 s4, 0x6000\n\t"
    "buffer_load_dwordx4 v14, s[40:43], s4 offen lds\n\t"
    "s_add_u32 s11, s11, s31\n\t"
    "s_mul_i32 s31, s11, 0x24000\n\t"
    "s_lshl_b32 s11, s11, 10\n\t"
    "s_mul_i32 s30, s10, 0x1c00\n\t"
    "s_add_u32 s31, s31, s30\n\t"
    "s_add_u32 s31, s31, 0x601000\n\t"
    "v_add_u32 v18, s31, v203\n\t"
    "s_add_u32 m0, s33, 0x24000\n\t"
    "s_mul_i32 s4, s75, 0x30000\n\t"
    "buffer_load_dwordx4 v17, s[40:43], s4 offen lds\n\t"
    "s_lshl_b32 s16, s8, 11\n\t"
    "s_lshl_b32 s11, s12, 16\n\t"
    "s_add_u32 s11, s11, s16\n\t"
    "v_add_u32 v199, s11, v203\n\t"
    "s_waitcnt vmcnt(7)\n\t"
    "s_barrier\n\t"
    "s_add_u32 m0, s33, 0x8000\n\t"
    "s_lshl_b32 s4, s76, 7\n\t"
    "buffer_load_dwordx4 v13, s[36:39], s4 offen lds\n\t"
    "s_mov_b32 s18, 0xffff\n\t"
    "s_mov_b32 s19, 0xffff\n\t"
    "v_add_u32 v204, 0x4000, v203\n\t"
    "v_and_b32 v248, 15, v1\n\t"
    "v_lshlrev_b32 v248, 4, v248\n\t"
    "s_add_u32 m0, s33, 0x9000\n\t"
    "s_lshl_b32 s4, s76, 7\n\t"
    "s_add_u32 s4, s4, 0x8000\n\t"
    "buffer_load_dwordx4 v13, s[36:39], s4 offen lds\n\t"
    "v_lshrrev_b32 v249, 5, v1\n\t"
    "v_lshl_or_b32 v205, v249, 8, v248\n\t"
    "v_add_u32 v205, 0x8000, v205\n\t"
    "v_and_b32 v248, 27, v1\n\t"
    "v_cmp_eq_u32 vcc, 16, v248\n\t"
    "s_add_u32 m0, s33, 0xa000\n\t"
    "s_lshl_b32 s4, s76, 7\n\t"
    "s_add_u32 s4, s4, 0x10000\n\t"
    "buffer_load_dwordx4 v13, s[36:39], s4 offen lds\n\t"
    "v_mov_b32 v250, 0x3f803f80\n\t"
    "v_mov_b32 v206, 0xdddddddd\n\t"
    "v_cndmask_b32 v180, 0, v250, vcc\n\t"
    "v_mov_b32 v181, v180\n\t"
    "v_mov_b32 v182, v180\n\t"
    "s_add_u32 m0, s33, 0xb000\n\t"
    "s_lshl_b32 s4, s76, 7\n\t"
    "s_add_u32 s4, s4, 0x18000\n\t"
    "buffer_load_dwordx4 v13, s[36:39], s4 offen lds\n\t"
    "v_mov_b32 v183, v180\n\t"
    "v_mov_b32 v184, v180\n\t"
    "v_mov_b32 v185, v180\n\t"
    "v_mov_b32 v186, v180\n\t"
    "v_mov_b32 v187, v180\n\t"
    "s_add_u32 m0, s34, 0x25000\n\t"
    "s_mul_i32 s4, s76, 0x30000\n\t"
    "buffer_load_dwordx4 v14, s[40:43], s4 offen lds\n\t"
    "v_mov_b32 v188, v180\n\t"
    "v_mov_b32 v189, v180\n\t"
    "v_mov_b32 v190, v180\n\t"
    "v_mov_b32 v191, v180\n\t"
    "v_mov_b32 v160, 0\n\t"
    "s_add_u32 m0, s34, 0x25400\n\t"
    "s_mul_i32 s4, s76, 0x30000\n\t"
    "s_addk_i32 s4, 0x6000\n\t"
    "buffer_load_dwordx4 v14, s[40:43], s4 offen lds\n\t"
    "v_mov_b32 v161, 0\n\t"
    "v_mov_b32 v162, 0\n\t"
    "v_mov_b32 v163, 0\n\t"
    "v_mov_b32 v164, 0\n\t"
    "v_mov_b32 v165, 0\n\t"
    "s_add_u32 m0, s33, 0x27000\n\t"
    "s_mul_i32 s4, s76, 0x30000\n\t"
    "buffer_load_dwordx4 v17, s[40:43], s4 offen lds\n\t"
    "v_mov_b32 v166, 0\n\t"
    "v_mov_b32 v167, 0\n\t"
    "v_mov_b32 v168, 0\n\t"
    "v_mov_b32 v169, 0\n\t"
    "v_mov_b32 v170, 0\n\t"
    "s_waitcnt vmcnt(7)\n\t"
    "s_barrier\n\t"
    "s_add_u32 m0, s33, 0xc000\n\t"
    "s_lshl_b32 s4, s77, 7\n\t"
    "buffer_load_dwordx4 v13, s[36:39], s4 offen lds\n\t"
    "v_mov_b32 v171, 0\n\t"
    "v_mov_b32 v172, 0\n\t"
    "v_mov_b32 v173, 0\n\t"
    "v_mov_b32 v174, 0\n\t"
    "v_mov_b32 v175, 0\n\t"
    "s_add_u32 m0, s33, 0xd000\n\t"
    "s_lshl_b32 s4, s77, 7\n\t"
    "s_add_u32 s4, s4, 0x8000\n\t"
    "buffer_load_dwordx4 v13, s[36:39], s4 offen lds\n\t"
    "v_bfe_u32 v248, v1, 4, 1\n\t"
    "v_lshlrev_b32 v248, 2, v248\n\t"
    "v_xor_b32 v248, v248, v1\n\t"
    "v_and_b32 v248, 7, v248\n\t"
    "v_lshrrev_b32 v250, 3, v1\n\t"
    "s_add_u32 m0, s33, 0xe000\n\t"
    "s_lshl_b32 s4, s77, 7\n\t"
    "s_add_u32 s4, s4, 0x10000\n\t"
    "buffer_load_dwordx4 v13, s[36:39], s4 offen lds\n\t"
    "v_lshlrev_b32 v250, 10, v250\n\t"
    "v_lshl_or_b32 v207, v248, 4, v250\n\t"
    "s_and_b32 s17, s10, 7\n\t"
    "s_lshl_b32 s17, s17, 7\n\t"
    "s_lshl_b32 s26, s8, 14\n\t"
    "s_add_u32 m0, s33, 0xf000\n\t"
    "s_lshl_b32 s4, s77, 7\n\t"
    "s_add_u32 s4, s4, 0x18000\n\t"
    "buffer_load_dwordx4 v13, s[36:39], s4 offen lds\n\t"
    "s_add_u32 s17, s17, s26\n\t"
    "v_add_u32 v207, s17, v207\n\t"
    "s_and_b32 s15, s8, 3\n\t"
    "s_lshl_b32 s15, s15, 12\n\t"
    "s_lshr_b32 s17, s8, 2\n\t"
    "s_add_u32 m0, s34, 0x19000\n\t"
    "s_mul_i32 s4, s77, 0x30000\n\t"
    "buffer_load_dwordx4 v14, s[40:43], s4 offen lds\n\t"
    "s_mul_i32 s17, s17, 0x24000\n\t"
    "s_add_u32 s15, s15, s17\n\t"
    "s_lshl_b32 s27, s8, 13\n\t"
    "s_add_u32 s27, s27, 0x4000\n\t"
    "s_lshl_b32 s29, s8, 12\n\t"
    "s_add_u32 m0, s34, 0x19400\n\t"
    "s_mul_i32 s4, s77, 0x30000\n\t"
    "s_addk_i32 s4, 0x6000\n\t"
    "buffer_load_dwordx4 v14, s[40:43], s4 offen lds\n\t"
    "s_add_u32 s29, s29, 0x14000\n\t"
    "v_bfe_u32 v248, v1, 2, 2\n\t"
    "v_lshl_add_u32 v248, v249, 3, v248\n\t"
    "v_lshlrev_b32 v252, 7, v248\n\t"
    "v_bfe_u32 v248, v1, 3, 1\n\t"
    "s_add_u32 m0, s33, 0x1b000\n\t"
    "s_mul_i32 s4, s77, 0x30000\n\t"
    "buffer_load_dwordx4 v17, s[40:43], s4 offen lds\n\t"
    "v_lshl_or_b32 v252, v248, 6, v252\n\t"
    "v_bfe_u32 v248, v1, 4, 1\n\t"
    "v_lshl_or_b32 v252, v248, 5, v252\n\t"
    "v_and_b32 v248, 1, v1\n\t"
    "v_lshl_or_b32 v252, v248, 4, v252\n\t"
    "s_mul_i32 s4, s77, 0x38000\n\t"
    "s_add_u32 m0, s35, 0x14000\n\t"
    "s_mov_b64 exec, s[72:73]\n\t"
    "buffer_load_dwordx4 v15, s[44:47], s4 offen lds\n\t"
    "s_mov_b64 exec, -1\n\t"
    "s_mul_i32 s4, s77, 0x38000\n\t"
    "s_add_u32 m0, s35, 0x13900\n\t"
    "s_mov_b64 exec, s[72:73]\n\t"
    "buffer_load_dwordx4 v22, s[44:47], s4 offen lds\n\t"
    "s_mov_b64 exec, -1\n\t"
    "s_mul_i32 s4, s77, 0x38000\n\t"
    "s_add_u32 m0, s35, 0x15c80\n\t"
    "s_mov_b64 exec, s[72:73]\n\t"
    "buffer_load_dwordx4 v15, s[48:51], s4 offen lds\n\t"
    "s_mov_b64 exec, -1\n\t"
    "s_mul_i32 s4, s77, 0x38000\n\t"
    "s_add_u32 m0, s35, 0x15580\n\t"
    "s_mov_b64 exec, s[72:73]\n\t"
    "buffer_load_dwordx4 v22, s[48:51], s4 offen lds\n\t"
    "s_mov_b64 exec, -1\n\t"
    "s_waitcnt vmcnt(11)\n\t"
    "s_barrier\n\t"
    "s_add_u32 m0, s33, 0x10000\n\t"
    "s_lshl_b32 s4, s78, 7\n\t"
    "buffer_load_dwordx4 v13, s[36:39], s4 offen lds\n\t"
    "v_bfe_u32 v248, v1, 1, 1\n\t"
    "v_lshl_or_b32 v252, v248, 3, v252\n\t"
    "v_add_u32 v252, s15, v252\n\t"
    "v_xor_b32 v253, 64, v252\n\t"
    "s_mul_i32 s17, s14, 0x120\n\t"
    "s_add_u32 m0, s33, 0x11000\n\t"
    "s_lshl_b32 s4, s78, 7\n\t"
    "s_add_u32 s4, s4, 0x8000\n\t"
    "buffer_load_dwordx4 v13, s[36:39], s4 offen lds\n\t"
    "s_add_u32 s17, s17, s8\n\t"
    "s_lshl_b32 s17, s17, 10\n\t"
    "s_add_u32 s17, s17, 0x600000\n\t"
    "v_add_u32 v20, s17, v203\n\t"
    "s_lshl_b32 s5, s14, 1\n\t"
    "s_add_u32 m0, s33, 0x12000\n\t"
    "s_lshl_b32 s4, s78, 7\n\t"
    "s_add_u32 s4, s4, 0x10000\n\t"
    "buffer_load_dwordx4 v13, s[36:39], s4 offen lds\n\t"
    "s_lshr_b32 s6, s8, 2\n\t"
    "s_add_u32 s5, s5, s6\n\t"
    "s_lshl_b32 s5, s5, 15\n\t"
    "s_and_b32 s6, s10, 7\n\t"
    "s_lshl_b32 s6, s6, 7\n\t"
    "s_add_u32 m0, s33, 0x13000\n\t"
    "s_lshl_b32 s4, s78, 7\n\t"
    "s_add_u32 s4, s4, 0x18000\n\t"
    "buffer_load_dwordx4 v13, s[36:39], s4 offen lds\n\t"
    "s_add_u32 s5, s5, s6\n\t"
    "s_and_b32 s6, s8, 3\n\t"
    "s_lshl_b32 s6, s6, 5\n\t"
    "s_add_u32 s5, s5, s6\n\t"
    "v_and_b32 v248, 31, v1\n\t"
    "s_add_u32 m0, s34, 0x1c000\n\t"
    "s_mul_i32 s4, s78, 0x30000\n\t"
    "buffer_load_dwordx4 v14, s[40:43], s4 offen lds\n\t"
    "v_lshlrev_b32 v248, 10, v248\n\t"
    "v_lshrrev_b32 v249, 5, v1\n\t"
    "v_lshl_or_b32 v21, v249, 4, v248\n\t"
    "v_add_u32 v21, s5, v21\n\t"
    "v_lshrrev_b32 v208, 2, v1\n\t"
    "s_add_u32 m0, s34, 0x1c400\n\t"
    "s_mul_i32 s4, s78, 0x30000\n\t"
    "s_addk_i32 s4, 0x6000\n\t"
    "buffer_load_dwordx4 v14, s[40:43], s4 offen lds\n\t"
    "v_mul_u32_u24 v208, 0xe0, v208\n\t"
    "v_and_b32 v209, 3, v1\n\t"
    "v_lshl_add_u32 v12, v209, 3, v208\n\t"
    "v_add_u32 v12, 0x14080, v12\n\t"
    "v_add_u32 v255, 0xab00, v12\n\t"
    "s_add_u32 m0, s33, 0x1e000\n\t"
    "s_mul_i32 s4, s78, 0x30000\n\t"
    "buffer_load_dwordx4 v17, s[40:43], s4 offen lds\n\t"
    "v_bfe_u32 v208, v1, 1, 3\n\t"
    "v_lshrrev_b32 v209, 4, v1\n\t"
    "v_xor_b32 v208, v208, v209\n\t"
    "v_lshlrev_b32 v208, 4, v208\n\t"
    "v_and_b32 v209, 15, v1\n\t"
    "s_waitcnt vmcnt(11)\n\t"
    "s_barrier\n\t"
    "s_add_u32 m0, s33, 0\n\t"
    "s_lshl_b32 s4, s79, 7\n\t"
    "buffer_load_dwordx4 v13, s[36:39], s4 offen lds\n\t"
    "v_lshl_or_b32 v248, v209, 7, v208\n\t"
    "s_and_b32 s5, s8, 3\n\t"
    "s_lshl_b32 s5, s5, 12\n\t"
    "v_add_u32 v248, s5, v248\n\t"
    "v_xor_b32 v249, 64, v248\n\t"
    "s_add_u32 m0, s33, 0x1000\n\t"
    "s_lshl_b32 s4, s79, 7\n\t"
    "s_add_u32 s4, s4, 0x8000\n\t"
    "buffer_load_dwordx4 v13, s[36:39], s4 offen lds\n\t"
    "v_add_u32 v250, 0x10000, v248\n\t"
    "v_add_u32 v251, 0x10000, v249\n\t"
    "s_add_u32 m0, s33, 0x2000\n\t"
    "s_lshl_b32 s4, s79, 7\n\t"
    "s_add_u32 s4, s4, 0x10000\n\t"
    "buffer_load_dwordx4 v13, s[36:39], s4 offen lds\n\t"
    "s_add_u32 m0, s33, 0x3000\n\t"
    "s_lshl_b32 s4, s79, 7\n\t"
    "s_add_u32 s4, s4, 0x18000\n\t"
    "buffer_load_dwordx4 v13, s[36:39], s4 offen lds\n\t"
    "s_add_u32 m0, s34, 0x1f000\n\t"
    "s_mul_i32 s4, s79, 0x30000\n\t"
    "buffer_load_dwordx4 v14, s[40:43], s4 offen lds\n\t"
    "s_add_u32 m0, s34, 0x1f400\n\t"
    "s_mul_i32 s4, s79, 0x30000\n\t"
    "s_addk_i32 s4, 0x6000\n\t"
    "buffer_load_dwordx4 v14, s[40:43], s4 offen lds\n\t"
    "s_add_u32 m0, s33, 0x21000\n\t"
    "s_mul_i32 s4, s79, 0x30000\n\t"
    "buffer_load_dwordx4 v17, s[40:43], s4 offen lds\n\t"
    "s_waitcnt vmcnt(7)\n\t"
    "s_barrier\n\t"
    "s_add_u32 m0, s33, 0x4000\n\t"
    "s_lshl_b32 s4, s80, 7\n\t"
    "buffer_load_dwordx4 v13, s[36:39], s4 offen lds\n\t"
    "s_add_u32 m0, s33, 0x5000\n\t"
    "s_lshl_b32 s4, s80, 7\n\t"
    "s_add_u32 s4, s4, 0x8000\n\t"
    "buffer_load_dwordx4 v13, s[36:39], s4 offen lds\n\t"
    "s_add_u32 m0, s33, 0x6000\n\t"
    "s_lshl_b32 s4, s80, 7\n\t"
    "s_add_u32 s4, s4, 0x10000\n\t"
    "buffer_load_dwordx4 v13, s[36:39], s4 offen lds\n\t"
    "s_add_u32 m0, s33, 0x7000\n\t"
    "s_lshl_b32 s4, s80, 7\n\t"
    "s_add_u32 s4, s4, 0x18000\n\t"
    "buffer_load_dwordx4 v13, s[36:39], s4 offen lds\n\t"
    "s_add_u32 m0, s34, 0x22000\n\t"
    "s_mul_i32 s4, s80, 0x30000\n\t"
    "buffer_load_dwordx4 v14, s[40:43], s4 offen lds\n\t"
    "s_add_u32 m0, s34, 0x22400\n\t"
    "s_mul_i32 s4, s80, 0x30000\n\t"
    "s_addk_i32 s4, 0x6000\n\t"
    "buffer_load_dwordx4 v14, s[40:43], s4 offen lds\n\t"
    "s_add_u32 m0, s33, 0x24000\n\t"
    "s_mul_i32 s4, s80, 0x30000\n\t"
    "buffer_load_dwordx4 v17, s[40:43], s4 offen lds\n\t"
    "s_waitcnt vmcnt(7)\n\t"
    "s_barrier\n\t"
    "s_add_u32 m0, s33, 0x8000\n\t"
    "s_lshl_b32 s4, s81, 7\n\t"
    "buffer_load_dwordx4 v13, s[36:39], s4 offen lds\n\t"
    "s_add_u32 m0, s33, 0x9000\n\t"
    "s_lshl_b32 s4, s81, 7\n\t"
    "s_add_u32 s4, s4, 0x8000\n\t"
    "buffer_load_dwordx4 v13, s[36:39], s4 offen lds\n\t"
    "s_add_u32 m0, s33, 0xa000\n\t"
    "s_lshl_b32 s4, s81, 7\n\t"
    "s_add_u32 s4, s4, 0x10000\n\t"
    "buffer_load_dwordx4 v13, s[36:39], s4 offen lds\n\t"
    "s_add_u32 m0, s33, 0xb000\n\t"
    "s_lshl_b32 s4, s81, 7\n\t"
    "s_add_u32 s4, s4, 0x18000\n\t"
    "buffer_load_dwordx4 v13, s[36:39], s4 offen lds\n\t"
    "s_add_u32 m0, s34, 0x25000\n\t"
    "s_mul_i32 s4, s81, 0x30000\n\t"
    "buffer_load_dwordx4 v14, s[40:43], s4 offen lds\n\t"
    "s_add_u32 m0, s34, 0x25400\n\t"
    "s_mul_i32 s4, s81, 0x30000\n\t"
    "s_addk_i32 s4, 0x6000\n\t"
    "buffer_load_dwordx4 v14, s[40:43], s4 offen lds\n\t"
    "s_add_u32 m0, s33, 0x27000\n\t"
    "s_mul_i32 s4, s81, 0x30000\n\t"
    "buffer_load_dwordx4 v17, s[40:43], s4 offen lds\n\t"
    "s_lshl_b32 s11, s9, 4\n\t"
    "s_add_u32 s11, s11, s10\n\t"
    "s_lshl_b32 s11, s11, 2\n\t"
    "s_and_b32 s30, s8, 3\n\t"
    "s_add_u32 s30, s11, s30\n\t"
    "s_lshl_b32 s11, s30, 11\n\t"
    "buffer_load_dwordx4 v[232:235], v203, s[36:39], s11 offen\n\t"
    "buffer_load_dwordx4 v[236:239], v203, s[36:39], s11 offen offset:1024\n\t"
    "buffer_load_dwordx4 v[240:243], v203, s[68:71], 0 offen\n\t"
    "s_lshl_b32 s30, s30, 3\n\t"
    "s_add_u32 s30, s30, 0x1000000\n\t"
    "v_mov_b32 v246, s30\n\t"
    "v_mov_b32 v208, -1\n\t"
    "v_mov_b32 v209, -1\n\t"
    "v_mov_b32 v210, -1\n\t"
    "v_mov_b32 v211, -1\n\t"
    "buffer_store_dwordx4 v[208:211], v19, s[52:55], 0 offen sc1\n\t"
    "s_mul_i32 s4, s77, 0x38000\n\t"
    "s_add_u32 s4, s4, 0x1c000\n\t"
    "s_add_u32 m0, s35, 0x17900\n\t"
    "s_mov_b64 exec, s[72:73]\n\t"
    "buffer_load_dwordx4 v15, s[44:47], s4 offen lds\n\t"
    "s_mov_b64 exec, -1\n\t"
    "s_mul_i32 s4, s77, 0x38000\n\t"
    "s_add_u32 s4, s4, 0x1c000\n\t"
    "s_add_u32 m0, s35, 0x17200\n\t"
    "s_mov_b64 exec, s[72:73]\n\t"
    "buffer_load_dwordx4 v22, s[44:47], s4 offen lds\n\t"
    "s_mov_b64 exec, -1\n\t"
    "s_mul_i32 s4, s77, 0x38000\n\t"
    "s_add_u32 s4, s4, 0x1c000\n\t"
    "s_add_u32 m0, s35, 0x19580\n\t"
    "s_mov_b64 exec, s[72:73]\n\t"
    "buffer_load_dwordx4 v15, s[48:51], s4 offen lds\n\t"
    "s_mov_b64 exec, -1\n\t"
    "s_mul_i32 s4, s77, 0x38000\n\t"
    "s_add_u32 s4, s4, 0x1c000\n\t"
    "s_add_u32 m0, s35, 0x18e80\n\t"
    "s_mov_b64 exec, s[72:73]\n\t"
    "buffer_load_dwordx4 v22, s[48:51], s4 offen lds\n\t"
    "s_mov_b64 exec, -1\n\t"
    "s_waitcnt vmcnt(15)\n\t"
    "s_barrier\n\t"
    "s_mul_i32 s4, s78, 0x38000\n\t"
    "s_add_u32 m0, s35, 0x1b200\n\t"
    "s_mov_b64 exec, s[72:73]\n\t"
    "buffer_load_dwordx4 v15, s[44:47], s4 offen lds\n\t"
    "s_mov_b64 exec, -1\n\t"
    "s_mul_i32 s4, s78, 0x38000\n\t"
    "s_add_u32 m0, s35, 0x1ab00\n\t"
    "s_mov_b64 exec, s[72:73]\n\t"
    "buffer_load_dwordx4 v22, s[44:47], s4 offen lds\n\t"
    "s_mov_b64 exec, -1\n\t"
    "s_mul_i32 s4, s78, 0x38000\n\t"
    "s_add_u32 m0, s35, 0x1ce80\n\t"
    "s_mov_b64 exec, s[72:73]\n\t"
    "buffer_load_dwordx4 v15, s[48:51], s4 offen lds\n\t"
    "s_mov_b64 exec, -1\n\t"
    "s_mul_i32 s4, s78, 0x38000\n\t"
    "s_add_u32 m0, s35, 0x1c780\n\t"
    "s_mov_b64 exec, s[72:73]\n\t"
    "buffer_load_dwordx4 v22, s[48:51], s4 offen lds\n\t"
    "s_mov_b64 exec, -1\n\t"
    "s_waitcnt vmcnt(12)\n\t"
    "s_barrier\n\t"
    "s_waitcnt vmcnt(9)\n\t"
    "v_dot2_f32_bf16 v244, v232, v240, 0\n\t"
    "v_dot2_f32_bf16 v245, v236, v240, 0\n\t"
    "v_dot2_f32_bf16 v244, v233, v241, v244\n\t"
    "v_dot2_f32_bf16 v245, v237, v241, v245\n\t"
    "v_dot2_f32_bf16 v244, v234, v242, v244\n\t"
    "v_dot2_f32_bf16 v245, v238, v242, v245\n\t"
    "v_dot2_f32_bf16 v244, v235, v243, v244\n\t"
    "v_dot2_f32_bf16 v245, v239, v243, v245\n\t"
    "s_nop 2\n\t"
    "v_add_f32_dpp v244, v244, v244 row_shr:1 row_mask:0xf bank_mask:0xf bound_ctrl:0\n\t"
    "v_add_f32_dpp v245, v245, v245 row_shr:1 row_mask:0xf bank_mask:0xf bound_ctrl:0\n\t"
    "s_nop 0\n\t"
    "v_add_f32_dpp v244, v244, v244 row_shr:2 row_mask:0xf bank_mask:0xf bound_ctrl:0\n\t"
    "v_add_f32_dpp v245, v245, v245 row_shr:2 row_mask:0xf bank_mask:0xf bound_ctrl:0\n\t"
    "s_nop 0\n\t"
    "v_add_f32_dpp v244, v244, v244 row_shr:4 row_mask:0xf bank_mask:0xf bound_ctrl:0\n\t"
    "v_add_f32_dpp v245, v245, v245 row_shr:4 row_mask:0xf bank_mask:0xf bound_ctrl:0\n\t"
    "s_nop 0\n\t"
    "v_add_f32_dpp v244, v244, v244 row_shr:8 row_mask:0xf bank_mask:0xf bound_ctrl:0\n\t"
    "v_add_f32_dpp v245, v245, v245 row_shr:8 row_mask:0xf bank_mask:0xf bound_ctrl:0\n\t"
    "s_nop 0\n\t"
    "v_add_f32_dpp v244, v244, v244 row_bcast:15 row_mask:0xa bank_mask:0xf\n\t"
    "v_add_f32_dpp v245, v245, v245 row_bcast:15 row_mask:0xa bank_mask:0xf\n\t"
    "s_nop 0\n\t"
    "v_add_f32_dpp v244, v244, v244 row_bcast:31 row_mask:0xc bank_mask:0xf\n\t"
    "v_add_f32_dpp v245, v245, v245 row_bcast:31 row_mask:0xc bank_mask:0xf\n\t"
    "v_mov_b32 v232, 0x422d1f97\n\t"
    "v_mul_f32 v244, 0x3fb8aa3b, v244\n\t"
    "v_mul_f32 v245, 0x3fb8aa3b, v245\n\t"
    "v_med3_f32 v234, v244, -v232, v232\n\t"
    "v_med3_f32 v235, v245, -v232, v232\n\t"
    "v_cmp_u_f32_e64 s[24:25], v244, v244\n\t"
    "s_nop 1\n\t"
    "v_cndmask_b32_e64 v244, v234, v244, s[24:25]\n\t"
    "v_cmp_u_f32_e64 s[24:25], v245, v245\n\t"
    "s_nop 1\n\t"
    "v_cndmask_b32_e64 v245, v235, v245, s[24:25]\n\t"
    "v_exp_f32 v244, v244\n\t"
    "v_exp_f32 v245, v245\n\t"
    "v_add_f32 v234, 1.0, v244\n\t"
    "v_add_f32 v235, 1.0, v245\n\t"
    "v_rcp_f32 v234, v234\n\t"
    "v_rcp_f32 v235, v235\n\t"
    "s_mov_b32 exec_lo, 0\n\t"
    "s_mov_b32 exec_hi, 0x80000000\n\t"
    "s_movk_i32 s11, 0x2000\n\t"
    "buffer_store_dwordx2 v[244:245], v246, s[52:55], 0 offen sc1\n\t"
    "buffer_store_dwordx2 v[234:235], v246, s[52:55], s11 offen sc1\n\t"
    "s_mov_b64 exec, -1\n\t"
    "s_waitcnt vmcnt(6) lgkmcnt(0)\n\t"
    "s_add_u32 s82, s82, 1\n\t"
    "s_barrier\n\t"
    "ds_read_b128 v[24:27], v248 offset:49152\n\t"
    "ds_read_b128 v[28:31], v248 offset:51200\n\t"
    "ds_read_b64_tr_b16 v[32:33], v12\n\t"
    "ds_read_b64_tr_b16 v[34:35], v12 offset:3584\n\t"
    "ds_read_b64_tr_b16 v[36:37], v12 offset:7296\n\t"
    "ds_read_b64_tr_b16 v[38:39], v12 offset:10880\n\t"
    "ds_read_b64_tr_b16 v[40:41], v12 offset:32\n\t"
    "ds_read_b64_tr_b16 v[42:43], v12 offset:3616\n\t"
    "ds_read_b64_tr_b16 v[44:45], v12 offset:7328\n\t"
    "ds_read_b64_tr_b16 v[46:47], v12 offset:10912\n\t"
    "ds_read_b64_tr_b16 v[48:49], v12 offset:64\n\t"
    "ds_read_b64_tr_b16 v[50:51], v12 offset:3648\n\t"
    "ds_read_b64_tr_b16 v[52:53], v12 offset:7360\n\t"
    "ds_read_b64_tr_b16 v[54:55], v12 offset:10944\n\t"
    "s_mul_i32 s4, s78, 0x38000\n\t"
    "s_add_u32 s4, s4, 0x1c000\n\t"
    "s_add_u32 m0, s35, 0x1eb00\n\t"
    "s_mov_b64 exec, s[72:73]\n\t"
    "buffer_load_dwordx4 v15, s[44:47], s4 offen lds\n\t"
    "s_mov_b64 exec, -1\n\t"
    "s_mul_i32 s4, s78, 0x38000\n\t"
    "s_add_u32 s4, s4, 0x1c000\n\t"
    "s_add_u32 m0, s35, 0x20780\n\t"
    "s_mov_b64 exec, s[72:73]\n\t"
    "buffer_load_dwordx4 v15, s[48:51], s4 offen lds\n\t"
    "s_mov_b64 exec, -1\n\t"
    "s_setprio 1\n\t"
    "s_waitcnt lgkmcnt(10)\n\t"
    "v_mfma_f32_16x16x32_bf16 v[64:67], v[32:35], v[24:27], 0\n\t"
    "ds_read_b128 v[216:219], v249 offset:49152\n\t"
    "ds_read_b128 v[220:223], v249 offset:51200\n\t"
    "v_mfma_f32_16x16x32_bf16 v[68:71], v[32:35], v[28:31], 0\n\t"
    "ds_read_b64_tr_b16 v[32:33], v12 offset:14592\n\t"
    "ds_read_b64_tr_b16 v[34:35], v12 offset:18176\n\t"
    "s_waitcnt lgkmcnt(12)\n\t"
    "v_mfma_f32_16x16x32_bf16 v[72:75], v[36:39], v[24:27], 0\n\t"
    "v_mfma_f32_16x16x32_bf16 v[76:79], v[36:39], v[28:31], 0\n\t"
    "ds_read_b64_tr_b16 v[36:37], v12 offset:21888\n\t"
    "ds_read_b64_tr_b16 v[38:39], v12 offset:25472\n\t"
    "s_waitcnt lgkmcnt(12)\n\t"
    "v_mfma_f32_16x16x32_bf16 v[80:83], v[40:43], v[24:27], 0\n\t"
    "s_mul_i32 s4, s79, 0x38000\n\t"
    "s_add_u32 m0, s35, 0x22400\n\t"
    "s_mov_b64 exec, s[72:73]\n\t"
    "buffer_load_dwordx4 v15, s[44:47], s4 offen lds\n\t"
    "s_mov_b64 exec, -1\n\t"
    "v_mfma_f32_16x16x32_bf16 v[84:87], v[40:43], v[28:31], 0\n\t"
    "ds_read_b64_tr_b16 v[40:41], v12 offset:14624\n\t"
    "ds_read_b64_tr_b16 v[42:43], v12 offset:18208\n\t"
    "s_waitcnt lgkmcnt(12)\n\t"
    "v_mfma_f32_16x16x32_bf16 v[88:91], v[44:47], v[24:27], 0\n\t"
    "v_mfma_f32_16x16x32_bf16 v[92:95], v[44:47], v[28:31], 0\n\t"
    "ds_read_b64_tr_b16 v[44:45], v12 offset:21920\n\t"
    "ds_read_b64_tr_b16 v[46:47], v12 offset:25504\n\t"
    "s_waitcnt lgkmcnt(12)\n\t"
    "v_mfma_f32_16x16x32_bf16 v[96:99], v[48:51], v[24:27], 0\n\t"
    "s_mul_i32 s4, s79, 0x38000\n\t"
    "s_add_u32 m0, s35, 0x24080\n\t"
    "s_mov_b64 exec, s[72:73]\n\t"
    "buffer_load_dwordx4 v15, s[48:51], s4 offen lds\n\t"
    "s_mov_b64 exec, -1\n\t"
    "v_mfma_f32_16x16x32_bf16 v[100:103], v[48:51], v[28:31], 0\n\t"
    "ds_read_b64_tr_b16 v[48:49], v12 offset:14656\n\t"
    "ds_read_b64_tr_b16 v[50:51], v12 offset:18240\n\t"
    "s_waitcnt lgkmcnt(12)\n\t"
    "v_mfma_f32_16x16x32_bf16 v[104:107], v[52:55], v[24:27], 0\n\t"
    "v_mfma_f32_16x16x32_bf16 v[108:111], v[52:55], v[28:31], 0\n\t"
    "ds_read_b64_tr_b16 v[52:53], v12 offset:21952\n\t"
    "ds_read_b64_tr_b16 v[54:55], v12 offset:25536\n\t"
    "s_waitcnt vmcnt(6)\n\t"
    "s_barrier\n\t"
    "s_waitcnt lgkmcnt(10)\n\t"
    "v_mfma_f32_16x16x32_bf16 v[64:67], v[32:35], v[216:219], v[64:67]\n\t"
    "ds_read_b128 v[24:27], v250\n\t"
    "ds_read_b128 v[28:31], v250 offset:2048\n\t"
    "v_mfma_f32_16x16x32_bf16 v[68:71], v[32:35], v[220:223], v[68:71]\n\t"
    "ds_read_b64_tr_b16 v[32:33], v12 offset:29184\n\t"
    "ds_read_b64_tr_b16 v[34:35], v12 offset:32768\n\t"
    "s_waitcnt lgkmcnt(12)\n\t"
    "v_mfma_f32_16x16x32_bf16 v[72:75], v[36:39], v[216:219], v[72:75]\n\t"
    "v_mfma_f32_16x16x32_bf16 v[76:79], v[36:39], v[220:223], v[76:79]\n\t"
    "ds_read_b64_tr_b16 v[36:37], v12 offset:36480\n\t"
    "ds_read_b64_tr_b16 v[38:39], v12 offset:40064\n\t"
    "s_waitcnt lgkmcnt(12)\n\t"
    "v_mfma_f32_16x16x32_bf16 v[80:83], v[40:43], v[216:219], v[80:83]\n\t"
    "s_mul_i32 s4, s79, 0x38000\n\t"
    "s_add_u32 s4, s4, 0x1c000\n\t"
    "s_add_u32 m0, s35, 0x14000\n\t"
    "s_mov_b64 exec, s[72:73]\n\t"
    "buffer_load_dwordx4 v15, s[44:47], s4 offen lds\n\t"
    "s_mov_b64 exec, -1\n\t"
    "v_mfma_f32_16x16x32_bf16 v[84:87], v[40:43], v[220:223], v[84:87]\n\t"
    "ds_read_b64_tr_b16 v[40:41], v12 offset:29216\n\t"
    "ds_read_b64_tr_b16 v[42:43], v12 offset:32800\n\t"
    "s_waitcnt lgkmcnt(12)\n\t"
    "v_mfma_f32_16x16x32_bf16 v[88:91], v[44:47], v[216:219], v[88:91]\n\t"
    "v_mfma_f32_16x16x32_bf16 v[92:95], v[44:47], v[220:223], v[92:95]\n\t"
    "ds_read_b64_tr_b16 v[44:45], v12 offset:36512\n\t"
    "ds_read_b64_tr_b16 v[46:47], v12 offset:40096\n\t"
    "s_waitcnt lgkmcnt(12)\n\t"
    "v_mfma_f32_16x16x32_bf16 v[96:99], v[48:51], v[216:219], v[96:99]\n\t"
    "s_mul_i32 s4, s79, 0x38000\n\t"
    "s_add_u32 s4, s4, 0x1c000\n\t"
    "s_add_u32 m0, s35, 0x15c80\n\t"
    "s_mov_b64 exec, s[72:73]\n\t"
    "buffer_load_dwordx4 v15, s[48:51], s4 offen lds\n\t"
    "s_mov_b64 exec, -1\n\t"
    "v_mfma_f32_16x16x32_bf16 v[100:103], v[48:51], v[220:223], v[100:103]\n\t"
    "ds_read_b64_tr_b16 v[48:49], v12 offset:29248\n\t"
    "ds_read_b64_tr_b16 v[50:51], v12 offset:32832\n\t"
    "s_waitcnt lgkmcnt(12)\n\t"
    "v_mfma_f32_16x16x32_bf16 v[104:107], v[52:55], v[216:219], v[104:107]\n\t"
    "v_mfma_f32_16x16x32_bf16 v[108:111], v[52:55], v[220:223], v[108:111]\n\t"
    "ds_read_b64_tr_b16 v[52:53], v12 offset:36544\n\t"
    "ds_read_b64_tr_b16 v[54:55], v12 offset:40128\n\t"
    "s_waitcnt vmcnt(4)\n\t"
    "s_barrier\n\t"
    "s_waitcnt lgkmcnt(10)\n\t"
    "v_mfma_f32_16x16x32_bf16 v[64:67], v[32:35], v[24:27], v[64:67]\n\t"
    "ds_read_b128 v[216:219], v251\n\t"
    "ds_read_b128 v[220:223], v251 offset:2048\n\t"
    "s_add_u32 m0, s33, 0xc000\n\t"
    "s_lshl_b32 s4, s74, 7\n\t"
    "buffer_load_dwordx4 v13, s[36:39], s4 offen lds\n\t"
    "v_mfma_f32_16x16x32_bf16 v[68:71], v[32:35], v[28:31], v[68:71]\n\t"
    "ds_read_b64_tr_b16 v[32:33], v255\n\t"
    "ds_read_b64_tr_b16 v[34:35], v255 offset:3584\n\t"
    "s_waitcnt lgkmcnt(12)\n\t"
    "v_mfma_f32_16x16x32_bf16 v[72:75], v[36:39], v[24:27], v[72:75]\n\t"
    "v_mfma_f32_16x16x32_bf16 v[76:79], v[36:39], v[28:31], v[76:79]\n\t"
    "ds_read_b64_tr_b16 v[36:37], v255 offset:7296\n\t"
    "ds_read_b64_tr_b16 v[38:39], v255 offset:10880\n\t"
    "s_waitcnt lgkmcnt(12)\n\t"
    "v_mfma_f32_16x16x32_bf16 v[80:83], v[40:43], v[24:27], v[80:83]\n\t"
    "s_mul_i32 s4, s80, 0x38000\n\t"
    "s_add_u32 m0, s35, 0x17900\n\t"
    "s_mov_b64 exec, s[72:73]\n\t"
    "buffer_load_dwordx4 v15, s[44:47], s4 offen lds\n\t"
    "s_mov_b64 exec, -1\n\t"
    "v_mfma_f32_16x16x32_bf16 v[84:87], v[40:43], v[28:31], v[84:87]\n\t"
    "ds_read_b64_tr_b16 v[40:41], v255 offset:32\n\t"
    "ds_read_b64_tr_b16 v[42:43], v255 offset:3616\n\t"
    "s_waitcnt lgkmcnt(12)\n\t"
    "v_mfma_f32_16x16x32_bf16 v[88:91], v[44:47], v[24:27], v[88:91]\n\t"
    "s_add_u32 m0, s33, 0xd000\n\t"
    "s_lshl_b32 s4, s74, 7\n\t"
    "s_add_u32 s4, s4, 0x8000\n\t"
    "buffer_load_dwordx4 v13, s[36:39], s4 offen lds\n\t"
    "v_mfma_f32_16x16x32_bf16 v[92:95], v[44:47], v[28:31], v[92:95]\n\t"
    "ds_read_b64_tr_b16 v[44:45], v255 offset:7328\n\t"
    "ds_read_b64_tr_b16 v[46:47], v255 offset:10912\n\t"
    "s_waitcnt lgkmcnt(12)\n\t"
    "v_mfma_f32_16x16x32_bf16 v[96:99], v[48:51], v[24:27], v[96:99]\n\t"
    "s_mul_i32 s4, s80, 0x38000\n\t"
    "s_add_u32 m0, s35, 0x19580\n\t"
    "s_mov_b64 exec, s[72:73]\n\t"
    "buffer_load_dwordx4 v15, s[48:51], s4 offen lds\n\t"
    "s_mov_b64 exec, -1\n\t"
    "v_mfma_f32_16x16x32_bf16 v[100:103], v[48:51], v[28:31], v[100:103]\n\t"
    "ds_read_b64_tr_b16 v[48:49], v255 offset:64\n\t"
    "ds_read_b64_tr_b16 v[50:51], v255 offset:3648\n\t"
    "s_waitcnt lgkmcnt(12)\n\t"
    "v_mfma_f32_16x16x32_bf16 v[104:107], v[52:55], v[24:27], v[104:107]\n\t"
    "s_add_u32 m0, s33, 0xe000\n\t"
    "s_lshl_b32 s4, s74, 7\n\t"
    "s_add_u32 s4, s4, 0x10000\n\t"
    "buffer_load_dwordx4 v13, s[36:39], s4 offen lds\n\t"
    "v_mfma_f32_16x16x32_bf16 v[108:111], v[52:55], v[28:31], v[108:111]\n\t"
    "ds_read_b64_tr_b16 v[52:53], v255 offset:7360\n\t"
    "ds_read_b64_tr_b16 v[54:55], v255 offset:10944\n\t"
    "s_waitcnt vmcnt(7)\n\t"
    "s_barrier\n\t"
    "s_waitcnt lgkmcnt(10)\n\t"
    "v_mfma_f32_16x16x32_bf16 v[64:67], v[32:35], v[216:219], v[64:67]\n\t"
    "ds_read_b128 v[24:27], v248\n\t"
    "ds_read_b128 v[28:31], v248 offset:2048\n\t"
    "s_add_u32 m0, s33, 0xf000\n\t"
    "s_lshl_b32 s4, s74, 7\n\t"
    "s_add_u32 s4, s4, 0x18000\n\t"
    "buffer_load_dwordx4 v13, s[36:39], s4 offen lds\n\t"
    "v_mfma_f32_16x16x32_bf16 v[68:71], v[32:35], v[220:223], v[68:71]\n\t"
    "ds_read_b64_tr_b16 v[32:33], v255 offset:14592\n\t"
    "ds_read_b64_tr_b16 v[34:35], v255 offset:18176\n\t"
    "s_waitcnt lgkmcnt(12)\n\t"
    "v_mfma_f32_16x16x32_bf16 v[72:75], v[36:39], v[216:219], v[72:75]\n\t"
    "v_mfma_f32_16x16x32_bf16 v[76:79], v[36:39], v[220:223], v[76:79]\n\t"
    "ds_read_b64_tr_b16 v[36:37], v255 offset:21888\n\t"
    "ds_read_b64_tr_b16 v[38:39], v255 offset:25472\n\t"
    "s_waitcnt lgkmcnt(12)\n\t"
    "v_mfma_f32_16x16x32_bf16 v[80:83], v[40:43], v[216:219], v[80:83]\n\t"
    "s_mul_i32 s4, s80, 0x38000\n\t"
    "s_add_u32 s4, s4, 0x1c000\n\t"
    "s_add_u32 m0, s35, 0x1b200\n\t"
    "s_mov_b64 exec, s[72:73]\n\t"
    "buffer_load_dwordx4 v15, s[44:47], s4 offen lds\n\t"
    "s_mov_b64 exec, -1\n\t"
    "v_mfma_f32_16x16x32_bf16 v[84:87], v[40:43], v[220:223], v[84:87]\n\t"
    "ds_read_b64_tr_b16 v[40:41], v255 offset:14624\n\t"
    "ds_read_b64_tr_b16 v[42:43], v255 offset:18208\n\t"
    "s_waitcnt lgkmcnt(12)\n\t"
    "v_mfma_f32_16x16x32_bf16 v[88:91], v[44:47], v[216:219], v[88:91]\n\t"
    "v_mfma_f32_16x16x32_bf16 v[92:95], v[44:47], v[220:223], v[92:95]\n\t"
    "ds_read_b64_tr_b16 v[44:45], v255 offset:21920\n\t"
    "ds_read_b64_tr_b16 v[46:47], v255 offset:25504\n\t"
    "s_waitcnt lgkmcnt(12)\n\t"
    "v_mfma_f32_16x16x32_bf16 v[96:99], v[48:51], v[216:219], v[96:99]\n\t"
    "s_mul_i32 s4, s80, 0x38000\n\t"
    "s_add_u32 s4, s4, 0x1c000\n\t"
    "s_add_u32 m0, s35, 0x1ce80\n\t"
    "s_mov_b64 exec, s[72:73]\n\t"
    "buffer_load_dwordx4 v15, s[48:51], s4 offen lds\n\t"
    "s_mov_b64 exec, -1\n\t"
    "v_mfma_f32_16x16x32_bf16 v[100:103], v[48:51], v[220:223], v[100:103]\n\t"
    "ds_read_b64_tr_b16 v[48:49], v255 offset:14656\n\t"
    "ds_read_b64_tr_b16 v[50:51], v255 offset:18240\n\t"
    "s_waitcnt lgkmcnt(12)\n\t"
    "v_mfma_f32_16x16x32_bf16 v[104:107], v[52:55], v[216:219], v[104:107]\n\t"
    "v_mfma_f32_16x16x32_bf16 v[108:111], v[52:55], v[220:223], v[108:111]\n\t"
    "ds_read_b64_tr_b16 v[52:53], v255 offset:21952\n\t"
    "ds_read_b64_tr_b16 v[54:55], v255 offset:25536\n\t"
    "s_waitcnt vmcnt(8)\n\t"
    "s_barrier\n\t"
    "s_waitcnt lgkmcnt(10)\n\t"
    "v_mfma_f32_16x16x32_bf16 v[64:67], v[32:35], v[24:27], v[64:67]\n\t"
    "ds_read_b128 v[216:219], v249\n\t"
    "ds_read_b128 v[220:223], v249 offset:2048\n\t"
    "s_add_u32 m0, s33, 0x10000\n\t"
    "s_lshl_b32 s4, s75, 7\n\t"
    "buffer_load_dwordx4 v13, s[36:39], s4 offen lds\n\t"
    "v_mfma_f32_16x16x32_bf16 v[68:71], v[32:35], v[28:31], v[68:71]\n\t"
    "ds_read_b64_tr_b16 v[32:33], v12\n\t"
    "ds_read_b64_tr_b16 v[34:35], v12 offset:3584\n\t"
    "s_waitcnt lgkmcnt(12)\n\t"
    "v_mfma_f32_16x16x32_bf16 v[72:75], v[36:39], v[24:27], v[72:75]\n\t"
    "v_mfma_f32_16x16x32_bf16 v[76:79], v[36:39], v[28:31], v[76:79]\n\t"
    "ds_read_b64_tr_b16 v[36:37], v12 offset:7296\n\t"
    "ds_read_b64_tr_b16 v[38:39], v12 offset:10880\n\t"
    "s_waitcnt lgkmcnt(12)\n\t"
    "v_mfma_f32_16x16x32_bf16 v[80:83], v[40:43], v[24:27], v[80:83]\n\t"
    "s_mul_i32 s4, s81, 0x38000\n\t"
    "s_add_u32 m0, s35, 0x1eb00\n\t"
    "s_mov_b64 exec, s[72:73]\n\t"
    "buffer_load_dwordx4 v15, s[44:47], s4 offen lds\n\t"
    "s_mov_b64 exec, -1\n\t"
    "v_mfma_f32_16x16x32_bf16 v[84:87], v[40:43], v[28:31], v[84:87]\n\t"
    "ds_read_b64_tr_b16 v[40:41], v12 offset:32\n\t"
    "ds_read_b64_tr_b16 v[42:43], v12 offset:3616\n\t"
    "s_waitcnt lgkmcnt(12)\n\t"
    "v_mfma_f32_16x16x32_bf16 v[88:91], v[44:47], v[24:27], v[88:91]\n\t"
    "s_add_u32 m0, s33, 0x11000\n\t"
    "s_lshl_b32 s4, s75, 7\n\t"
    "s_add_u32 s4, s4, 0x8000\n\t"
    "buffer_load_dwordx4 v13, s[36:39], s4 offen lds\n\t"
    "v_mfma_f32_16x16x32_bf16 v[92:95], v[44:47], v[28:31], v[92:95]\n\t"
    "ds_read_b64_tr_b16 v[44:45], v12 offset:7328\n\t"
    "ds_read_b64_tr_b16 v[46:47], v12 offset:10912\n\t"
    "s_waitcnt lgkmcnt(12)\n\t"
    "v_mfma_f32_16x16x32_bf16 v[96:99], v[48:51], v[24:27], v[96:99]\n\t"
    "s_mul_i32 s4, s81, 0x38000\n\t"
    "s_add_u32 m0, s35, 0x20780\n\t"
    "s_mov_b64 exec, s[72:73]\n\t"
    "buffer_load_dwordx4 v15, s[48:51], s4 offen lds\n\t"
    "s_mov_b64 exec, -1\n\t"
    "v_mfma_f32_16x16x32_bf16 v[100:103], v[48:51], v[28:31], v[100:103]\n\t"
    "ds_read_b64_tr_b16 v[48:49], v12 offset:64\n\t"
    "ds_read_b64_tr_b16 v[50:51], v12 offset:3648\n\t"
    "s_waitcnt lgkmcnt(12)\n\t"
    "v_mfma_f32_16x16x32_bf16 v[104:107], v[52:55], v[24:27], v[104:107]\n\t"
    "s_add_u32 m0, s33, 0x12000\n\t"
    "s_lshl_b32 s4, s75, 7\n\t"
    "s_add_u32 s4, s4, 0x10000\n\t"
    "buffer_load_dwordx4 v13, s[36:39], s4 offen lds\n\t"
    "v_mfma_f32_16x16x32_bf16 v[108:111], v[52:55], v[28:31], v[108:111]\n\t"
    "ds_read_b64_tr_b16 v[52:53], v12 offset:7360\n\t"
    "ds_read_b64_tr_b16 v[54:55], v12 offset:10944\n\t"
    "s_waitcnt vmcnt(9)\n\t"
    "s_barrier\n\t"
    "s_waitcnt lgkmcnt(10)\n\t"
    "v_mfma_f32_16x16x32_bf16 v[64:67], v[32:35], v[216:219], v[64:67]\n\t"
    "ds_read_b128 v[24:27], v248 offset:16384\n\t"
    "ds_read_b128 v[28:31], v248 offset:18432\n\t"
    "s_add_u32 m0, s33, 0x13000\n\t"
    "s_lshl_b32 s4, s75, 7\n\t"
    "s_add_u32 s4, s4, 0x18000\n\t"
    "buffer_load_dwordx4 v13, s[36:39], s4 offen lds\n\t"
    "v_mfma_f32_16x16x32_bf16 v[68:71], v[32:35], v[220:223], v[68:71]\n\t"
    "ds_read_b64_tr_b16 v[32:33], v12 offset:14592\n\t"
    "ds_read_b64_tr_b16 v[34:35], v12 offset:18176\n\t"
    "s_waitcnt lgkmcnt(12)\n\t"
    "v_mfma_f32_16x16x32_bf16 v[72:75], v[36:39], v[216:219], v[72:75]\n\t"
    "v_mfma_f32_16x16x32_bf16 v[76:79], v[36:39], v[220:223], v[76:79]\n\t"
    "ds_read_b64_tr_b16 v[36:37], v12 offset:21888\n\t"
    "ds_read_b64_tr_b16 v[38:39], v12 offset:25472\n\t"
    "s_waitcnt lgkmcnt(12)\n\t"
    "v_mfma_f32_16x16x32_bf16 v[80:83], v[40:43], v[216:219], v[80:83]\n\t"
    "s_mul_i32 s4, s81, 0x38000\n\t"
    "s_add_u32 s4, s4, 0x1c000\n\t"
    "s_add_u32 m0, s35, 0x22400\n\t"
    "s_mov_b64 exec, s[72:73]\n\t"
    "buffer_load_dwordx4 v15, s[44:47], s4 offen lds\n\t"
    "s_mov_b64 exec, -1\n\t"
    "v_mfma_f32_16x16x32_bf16 v[84:87], v[40:43], v[220:223], v[84:87]\n\t"
    "ds_read_b64_tr_b16 v[40:41], v12 offset:14624\n\t"
    "ds_read_b64_tr_b16 v[42:43], v12 offset:18208\n\t"
    "s_waitcnt lgkmcnt(12)\n\t"
    "v_mfma_f32_16x16x32_bf16 v[88:91], v[44:47], v[216:219], v[88:91]\n\t"
    "v_mfma_f32_16x16x32_bf16 v[92:95], v[44:47], v[220:223], v[92:95]\n\t"
    "ds_read_b64_tr_b16 v[44:45], v12 offset:21920\n\t"
    "ds_read_b64_tr_b16 v[46:47], v12 offset:25504\n\t"
    "s_waitcnt lgkmcnt(12)\n\t"
    "v_mfma_f32_16x16x32_bf16 v[96:99], v[48:51], v[216:219], v[96:99]\n\t"
    "s_mul_i32 s4, s81, 0x38000\n\t"
    "s_add_u32 s4, s4, 0x1c000\n\t"
    "s_add_u32 m0, s35, 0x24080\n\t"
    "s_mov_b64 exec, s[72:73]\n\t"
    "buffer_load_dwordx4 v15, s[48:51], s4 offen lds\n\t"
    "s_mov_b64 exec, -1\n\t"
    "v_mfma_f32_16x16x32_bf16 v[100:103], v[48:51], v[220:223], v[100:103]\n\t"
    "ds_read_b64_tr_b16 v[48:49], v12 offset:14656\n\t"
    "ds_read_b64_tr_b16 v[50:51], v12 offset:18240\n\t"
    "s_waitcnt lgkmcnt(12)\n\t"
    "v_mfma_f32_16x16x32_bf16 v[104:107], v[52:55], v[216:219], v[104:107]\n\t"
    "v_mfma_f32_16x16x32_bf16 v[108:111], v[52:55], v[220:223], v[108:111]\n\t"
    "ds_read_b64_tr_b16 v[52:53], v12 offset:21952\n\t"
    "ds_read_b64_tr_b16 v[54:55], v12 offset:25536\n\t"
    "s_waitcnt vmcnt(8)\n\t"
    "s_barrier\n\t"
    "s_waitcnt lgkmcnt(10)\n\t"
    "v_mfma_f32_16x16x32_bf16 v[64:67], v[32:35], v[24:27], v[64:67]\n\t"
    "ds_read_b128 v[216:219], v249 offset:16384\n\t"
    "ds_read_b128 v[220:223], v249 offset:18432\n\t"
    "s_add_u32 m0, s33, 0\n\t"
    "s_lshl_b32 s4, s76, 7\n\t"
    "buffer_load_dwordx4 v13, s[36:39], s4 offen lds\n\t"
    "v_mfma_f32_16x16x32_bf16 v[68:71], v[32:35], v[28:31], v[68:71]\n\t"
    "ds_read_b64_tr_b16 v[32:33], v12 offset:29184\n\t"
    "ds_read_b64_tr_b16 v[34:35], v12 offset:32768\n\t"
    "s_waitcnt lgkmcnt(12)\n\t"
    "v_mfma_f32_16x16x32_bf16 v[72:75], v[36:39], v[24:27], v[72:75]\n\t"
    "v_mfma_f32_16x16x32_bf16 v[76:79], v[36:39], v[28:31], v[76:79]\n\t"
    "ds_read_b64_tr_b16 v[36:37], v12 offset:36480\n\t"
    "ds_read_b64_tr_b16 v[38:39], v12 offset:40064\n\t"
    "s_waitcnt lgkmcnt(12)\n\t"
    "v_mfma_f32_16x16x32_bf16 v[80:83], v[40:43], v[24:27], v[80:83]\n\t"
    "s_mul_i32 s4, s74, 0x38000\n\t"
    "s_add_u32 m0, s35, 0x14000\n\t"
    "s_mov_b64 exec, s[72:73]\n\t"
    "buffer_load_dwordx4 v15, s[44:47], s4 offen lds\n\t"
    "s_mov_b64 exec, -1\n\t"
    "v_mfma_f32_16x16x32_bf16 v[84:87], v[40:43], v[28:31], v[84:87]\n\t"
    "ds_read_b64_tr_b16 v[40:41], v12 offset:29216\n\t"
    "ds_read_b64_tr_b16 v[42:43], v12 offset:32800\n\t"
    "s_waitcnt lgkmcnt(12)\n\t"
    "v_mfma_f32_16x16x32_bf16 v[88:91], v[44:47], v[24:27], v[88:91]\n\t"
    "s_add_u32 m0, s33, 0x1000\n\t"
    "s_lshl_b32 s4, s76, 7\n\t"
    "s_add_u32 s4, s4, 0x8000\n\t"
    "buffer_load_dwordx4 v13, s[36:39], s4 offen lds\n\t"
    "v_mfma_f32_16x16x32_bf16 v[92:95], v[44:47], v[28:31], v[92:95]\n\t"
    "ds_read_b64_tr_b16 v[44:45], v12 offset:36512\n\t"
    "ds_read_b64_tr_b16 v[46:47], v12 offset:40096\n\t"
    "s_waitcnt lgkmcnt(12)\n\t"
    "v_mfma_f32_16x16x32_bf16 v[96:99], v[48:51], v[24:27], v[96:99]\n\t"
    "s_mul_i32 s4, s74, 0x38000\n\t"
    "s_add_u32 m0, s35, 0x15c80\n\t"
    "s_mov_b64 exec, s[72:73]\n\t"
    "buffer_load_dwordx4 v15, s[48:51], s4 offen lds\n\t"
    "s_mov_b64 exec, -1\n\t"
    "v_mfma_f32_16x16x32_bf16 v[100:103], v[48:51], v[28:31], v[100:103]\n\t"
    "ds_read_b64_tr_b16 v[48:49], v12 offset:29248\n\t"
    "ds_read_b64_tr_b16 v[50:51], v12 offset:32832\n\t"
    "s_waitcnt lgkmcnt(12)\n\t"
    "v_mfma_f32_16x16x32_bf16 v[104:107], v[52:55], v[24:27], v[104:107]\n\t"
    "s_add_u32 m0, s33, 0x2000\n\t"
    "s_lshl_b32 s4, s76, 7\n\t"
    "s_add_u32 s4, s4, 0x10000\n\t"
    "buffer_load_dwordx4 v13, s[36:39], s4 offen lds\n\t"
    "v_mfma_f32_16x16x32_bf16 v[108:111], v[52:55], v[28:31], v[108:111]\n\t"
    "ds_read_b64_tr_b16 v[52:53], v12 offset:36544\n\t"
    "ds_read_b64_tr_b16 v[54:55], v12 offset:40128\n\t"
    "s_waitcnt vmcnt(9)\n\t"
    "s_barrier\n\t"
    "s_waitcnt lgkmcnt(10)\n\t"
    "v_mfma_f32_16x16x32_bf16 v[64:67], v[32:35], v[216:219], v[64:67]\n\t"
    "ds_read_b128 v[24:27], v248 offset:32768\n\t"
    "ds_read_b128 v[28:31], v248 offset:34816\n\t"
    "s_add_u32 m0, s33, 0x3000\n\t"
    "s_lshl_b32 s4, s76, 7\n\t"
    "s_add_u32 s4, s4, 0x18000\n\t"
    "buffer_load_dwordx4 v13, s[36:39], s4 offen lds\n\t"
    "v_mfma_f32_16x16x32_bf16 v[68:71], v[32:35], v[220:223], v[68:71]\n\t"
    "ds_read_b64_tr_b16 v[32:33], v255\n\t"
    "ds_read_b64_tr_b16 v[34:35], v255 offset:3584\n\t"
    "s_waitcnt lgkmcnt(12)\n\t"
    "v_mfma_f32_16x16x32_bf16 v[72:75], v[36:39], v[216:219], v[72:75]\n\t"
    "v_mfma_f32_16x16x32_bf16 v[76:79], v[36:39], v[220:223], v[76:79]\n\t"
    "ds_read_b64_tr_b16 v[36:37], v255 offset:7296\n\t"
    "ds_read_b64_tr_b16 v[38:39], v255 offset:10880\n\t"
    "s_waitcnt lgkmcnt(12)\n\t"
    "v_mfma_f32_16x16x32_bf16 v[80:83], v[40:43], v[216:219], v[80:83]\n\t"
    "s_mul_i32 s4, s74, 0x38000\n\t"
    "s_add_u32 s4, s4, 0x1c000\n\t"
    "s_add_u32 m0, s35, 0x17900\n\t"
    "s_mov_b64 exec, s[72:73]\n\t"
    "buffer_load_dwordx4 v15, s[44:47], s4 offen lds\n\t"
    "s_mov_b64 exec, -1\n\t"
    "v_mfma_f32_16x16x32_bf16 v[84:87], v[40:43], v[220:223], v[84:87]\n\t"
    "ds_read_b64_tr_b16 v[40:41], v255 offset:32\n\t"
    "ds_read_b64_tr_b16 v[42:43], v255 offset:3616\n\t"
    "s_waitcnt lgkmcnt(12)\n\t"
    "v_mfma_f32_16x16x32_bf16 v[88:91], v[44:47], v[216:219], v[88:91]\n\t"
    "v_mfma_f32_16x16x32_bf16 v[92:95], v[44:47], v[220:223], v[92:95]\n\t"
    "ds_read_b64_tr_b16 v[44:45], v255 offset:7328\n\t"
    "ds_read_b64_tr_b16 v[46:47], v255 offset:10912\n\t"
    "s_waitcnt lgkmcnt(12)\n\t"
    "v_mfma_f32_16x16x32_bf16 v[96:99], v[48:51], v[216:219], v[96:99]\n\t"
    "s_mul_i32 s4, s74, 0x38000\n\t"
    "s_add_u32 s4, s4, 0x1c000\n\t"
    "s_add_u32 m0, s35, 0x19580\n\t"
    "s_mov_b64 exec, s[72:73]\n\t"
    "buffer_load_dwordx4 v15, s[48:51], s4 offen lds\n\t"
    "s_mov_b64 exec, -1\n\t"
    "v_mfma_f32_16x16x32_bf16 v[100:103], v[48:51], v[220:223], v[100:103]\n\t"
    "ds_read_b64_tr_b16 v[48:49], v255 offset:64\n\t"
    "ds_read_b64_tr_b16 v[50:51], v255 offset:3648\n\t"
    "s_waitcnt lgkmcnt(12)\n\t"
    "v_mfma_f32_16x16x32_bf16 v[104:107], v[52:55], v[216:219], v[104:107]\n\t"
    "v_mfma_f32_16x16x32_bf16 v[108:111], v[52:55], v[220:223], v[108:111]\n\t"
    "ds_read_b64_tr_b16 v[52:53], v255 offset:7360\n\t"
    "ds_read_b64_tr_b16 v[54:55], v255 offset:10944\n\t"
    "s_waitcnt vmcnt(8)\n\t"
    "s_barrier\n\t"
    "s_waitcnt lgkmcnt(10)\n\t"
    "v_mfma_f32_16x16x32_bf16 v[64:67], v[32:35], v[24:27], v[64:67]\n\t"
    "ds_read_b128 v[216:219], v249 offset:32768\n\t"
    "ds_read_b128 v[220:223], v249 offset:34816\n\t"
    "v_mfma_f32_16x16x32_bf16 v[68:71], v[32:35], v[28:31], v[68:71]\n\t"
    "ds_read_b64_tr_b16 v[32:33], v255 offset:14592\n\t"
    "ds_read_b64_tr_b16 v[34:35], v255 offset:18176\n\t"
    "s_waitcnt lgkmcnt(12)\n\t"
    "v_mfma_f32_16x16x32_bf16 v[72:75], v[36:39], v[24:27], v[72:75]\n\t"
    "v_mfma_f32_16x16x32_bf16 v[76:79], v[36:39], v[28:31], v[76:79]\n\t"
    "ds_read_b64_tr_b16 v[36:37], v255 offset:21888\n\t"
    "ds_read_b64_tr_b16 v[38:39], v255 offset:25472\n\t"
    "s_waitcnt lgkmcnt(12)\n\t"
    "v_mfma_f32_16x16x32_bf16 v[80:83], v[40:43], v[24:27], v[80:83]\n\t"
    "s_mul_i32 s4, s75, 0x38000\n\t"
    "s_add_u32 m0, s35, 0x1b200\n\t"
    "s_mov_b64 exec, s[72:73]\n\t"
    "buffer_load_dwordx4 v15, s[44:47], s4 offen lds\n\t"
    "s_mov_b64 exec, -1\n\t"
    "v_mfma_f32_16x16x32_bf16 v[84:87], v[40:43], v[28:31], v[84:87]\n\t"
    "ds_read_b64_tr_b16 v[40:41], v255 offset:14624\n\t"
    "ds_read_b64_tr_b16 v[42:43], v255 offset:18208\n\t"
    "s_waitcnt lgkmcnt(12)\n\t"
    "v_mfma_f32_16x16x32_bf16 v[88:91], v[44:47], v[24:27], v[88:91]\n\t"
    "v_mfma_f32_16x16x32_bf16 v[92:95], v[44:47], v[28:31], v[92:95]\n\t"
    "ds_read_b64_tr_b16 v[44:45], v255 offset:21920\n\t"
    "ds_read_b64_tr_b16 v[46:47], v255 offset:25504\n\t"
    "s_waitcnt lgkmcnt(12)\n\t"
    "v_mfma_f32_16x16x32_bf16 v[96:99], v[48:51], v[24:27], v[96:99]\n\t"
    "s_mul_i32 s4, s75, 0x38000\n\t"
    "s_add_u32 m0, s35, 0x1ce80\n\t"
    "s_mov_b64 exec, s[72:73]\n\t"
    "buffer_load_dwordx4 v15, s[48:51], s4 offen lds\n\t"
    "s_mov_b64 exec, -1\n\t"
    "v_mfma_f32_16x16x32_bf16 v[100:103], v[48:51], v[28:31], v[100:103]\n\t"
    "ds_read_b64_tr_b16 v[48:49], v255 offset:14656\n\t"
    "ds_read_b64_tr_b16 v[50:51], v255 offset:18240\n\t"
    "s_waitcnt lgkmcnt(12)\n\t"
    "v_mfma_f32_16x16x32_bf16 v[104:107], v[52:55], v[24:27], v[104:107]\n\t"
    "v_mfma_f32_16x16x32_bf16 v[108:111], v[52:55], v[28:31], v[108:111]\n\t"
    "ds_read_b64_tr_b16 v[52:53], v255 offset:21952\n\t"
    "ds_read_b64_tr_b16 v[54:55], v255 offset:25536\n\t"
    "s_waitcnt vmcnt(6)\n\t"
    "s_barrier\n\t"
    "s_waitcnt lgkmcnt(10)\n\t"
    "v_mfma_f32_16x16x32_bf16 v[64:67], v[32:35], v[216:219], v[64:67]\n\t"
    "ds_read_b128 v[24:27], v248 offset:49152\n\t"
    "ds_read_b128 v[28:31], v248 offset:51200\n\t"
    "v_mfma_f32_16x16x32_bf16 v[68:71], v[32:35], v[220:223], v[68:71]\n\t"
    "ds_read_b64_tr_b16 v[32:33], v12\n\t"
    "ds_read_b64_tr_b16 v[34:35], v12 offset:3584\n\t"
    "s_waitcnt lgkmcnt(12)\n\t"
    "v_mfma_f32_16x16x32_bf16 v[72:75], v[36:39], v[216:219], v[72:75]\n\t"
    "v_mfma_f32_16x16x32_bf16 v[76:79], v[36:39], v[220:223], v[76:79]\n\t"
    "ds_read_b64_tr_b16 v[36:37], v12 offset:7296\n\t"
    "ds_read_b64_tr_b16 v[38:39], v12 offset:10880\n\t"
    "s_waitcnt lgkmcnt(12)\n\t"
    "v_mfma_f32_16x16x32_bf16 v[80:83], v[40:43], v[216:219], v[80:83]\n\t"
    "s_mul_i32 s4, s75, 0x38000\n\t"
    "s_add_u32 s4, s4, 0x1c000\n\t"
    "s_add_u32 m0, s35, 0x1eb00\n\t"
    "s_mov_b64 exec, s[72:73]\n\t"
    "buffer_load_dwordx4 v15, s[44:47], s4 offen lds\n\t"
    "s_mov_b64 exec, -1\n\t"
    "v_mfma_f32_16x16x32_bf16 v[84:87], v[40:43], v[220:223], v[84:87]\n\t"
    "ds_read_b64_tr_b16 v[40:41], v12 offset:32\n\t"
    "ds_read_b64_tr_b16 v[42:43], v12 offset:3616\n\t"
    "s_waitcnt lgkmcnt(12)\n\t"
    "v_mfma_f32_16x16x32_bf16 v[88:91], v[44:47], v[216:219], v[88:91]\n\t"
    "v_mfma_f32_16x16x32_bf16 v[92:95], v[44:47], v[220:223], v[92:95]\n\t"
    "ds_read_b64_tr_b16 v[44:45], v12 offset:7328\n\t"
    "ds_read_b64_tr_b16 v[46:47], v12 offset:10912\n\t"
    "s_waitcnt lgkmcnt(12)\n\t"
    "v_mfma_f32_16x16x32_bf16 v[96:99], v[48:51], v[216:219], v[96:99]\n\t"
    "s_mul_i32 s4, s75, 0x38000\n\t"
    "s_add_u32 s4, s4, 0x1c000\n\t"
    "s_add_u32 m0, s35, 0x20780\n\t"
    "s_mov_b64 exec, s[72:73]\n\t"
    "buffer_load_dwordx4 v15, s[48:51], s4 offen lds\n\t"
    "s_mov_b64 exec, -1\n\t"
    "v_mfma_f32_16x16x32_bf16 v[100:103], v[48:51], v[220:223], v[100:103]\n\t"
    "ds_read_b64_tr_b16 v[48:49], v12 offset:64\n\t"
    "ds_read_b64_tr_b16 v[50:51], v12 offset:3648\n\t"
    "s_waitcnt lgkmcnt(12)\n\t"
    "v_mfma_f32_16x16x32_bf16 v[104:107], v[52:55], v[216:219], v[104:107]\n\t"
    "v_mfma_f32_16x16x32_bf16 v[108:111], v[52:55], v[220:223], v[108:111]\n\t"
    "ds_read_b64_tr_b16 v[52:53], v12 offset:7360\n\t"
    "ds_read_b64_tr_b16 v[54:55], v12 offset:10944\n\t"
    "s_waitcnt vmcnt(4)\n\t"
    "s_barrier\n\t"
    "s_lshr_b32 s30, s12, 2\n\t"
    "s_lshl_b32 s30, s30, 8\n\t"
    "s_add_u32 s30, s30, 0x1004000\n\t"
    "v_and_b32 v202, 31, v1\n\t"
    "v_lshlrev_b32 v202, 3, v202\n\t"
    "v_add_u32 v202, s30, v202\n\t"
    "s_cmp_lg_u32 s8, 4\n\t"
    "s_cbranch_scc1 L_np_%=\n\t"
    "buffer_load_dwordx2 v[200:201], v202, s[52:55], 0 offen sc1\n\t"
    "L_np_%=:\n\t"
    "s_waitcnt lgkmcnt(10)\n\t"
    "v_mfma_f32_16x16x32_bf16 v[64:67], v[32:35], v[24:27], v[64:67]\n\t"
    "ds_read_b128 v[216:219], v249 offset:49152\n\t"
    "ds_read_b128 v[220:223], v249 offset:51200\n\t"
    "v_mfma_f32_16x16x32_bf16 v[68:71], v[32:35], v[28:31], v[68:71]\n\t"
    "ds_read_b64_tr_b16 v[32:33], v12 offset:14592\n\t"
    "ds_read_b64_tr_b16 v[34:35], v12 offset:18176\n\t"
    "s_waitcnt lgkmcnt(12)\n\t"
    "v_mfma_f32_16x16x32_bf16 v[72:75], v[36:39], v[24:27], v[72:75]\n\t"
    "v_mfma_f32_16x16x32_bf16 v[76:79], v[36:39], v[28:31], v[76:79]\n\t"
    "ds_read_b64_tr_b16 v[36:37], v12 offset:21888\n\t"
    "ds_read_b64_tr_b16 v[38:39], v12 offset:25472\n\t"
    "s_waitcnt lgkmcnt(12)\n\t"
    "v_mfma_f32_16x16x32_bf16 v[80:83], v[40:43], v[24:27], v[80:83]\n\t"
    "s_mul_i32 s4, s76, 0x38000\n\t"
    "s_add_u32 m0, s35, 0x22400\n\t"
    "s_mov_b64 exec, s[72:73]\n\t"
    "buffer_load_dwordx4 v15, s[44:47], s4 offen lds\n\t"
    "s_mov_b64 exec, -1\n\t"
    "v_mfma_f32_16x16x32_bf16 v[84:87], v[40:43], v[28:31], v[84:87]\n\t"
    "ds_read_b64_tr_b16 v[40:41], v12 offset:14624\n\t"
    "ds_read_b64_tr_b16 v[42:43], v12 offset:18208\n\t"
    "s_waitcnt lgkmcnt(12)\n\t"
    "v_mfma_f32_16x16x32_bf16 v[88:91], v[44:47], v[24:27], v[88:91]\n\t"
    "v_mfma_f32_16x16x32_bf16 v[92:95], v[44:47], v[28:31], v[92:95]\n\t"
    "ds_read_b64_tr_b16 v[44:45], v12 offset:21920\n\t"
    "ds_read_b64_tr_b16 v[46:47], v12 offset:25504\n\t"
    "s_waitcnt lgkmcnt(12)\n\t"
    "v_mfma_f32_16x16x32_bf16 v[96:99], v[48:51], v[24:27], v[96:99]\n\t"
    "s_mul_i32 s4, s76, 0x38000\n\t"
    "s_add_u32 m0, s35, 0x24080\n\t"
    "s_mov_b64 exec, s[72:73]\n\t"
    "buffer_load_dwordx4 v15, s[48:51], s4 offen lds\n\t"
    "s_mov_b64 exec, -1\n\t"
    "v_mfma_f32_16x16x32_bf16 v[100:103], v[48:51], v[28:31], v[100:103]\n\t"
    "ds_read_b64_tr_b16 v[48:49], v12 offset:14656\n\t"
    "ds_read_b64_tr_b16 v[50:51], v12 offset:18240\n\t"
    "s_waitcnt lgkmcnt(12)\n\t"
    "v_mfma_f32_16x16x32_bf16 v[104:107], v[52:55], v[24:27], v[104:107]\n\t"
    "v_mfma_f32_16x16x32_bf16 v[108:111], v[52:55], v[28:31], v[108:111]\n\t"
    "ds_read_b64_tr_b16 v[52:53], v12 offset:21952\n\t"
    "ds_read_b64_tr_b16 v[54:55], v12 offset:25536\n\t"
    "s_waitcnt vmcnt(2)\n\t"
    "v_cmp_ne_u64 vcc, s[82:83], v[200:201]\n\t"
    "s_cmp_eq_u32 s8, 4\n\t"
    "s_cselect_b64 s[24:25], -1, 0\n\t"
    "s_and_b64 vcc, vcc, s[24:25]\n\t"
    "s_cbranch_scc1 L_s1_retry_%=\n\t"
    "L_s1_ok_%=:\n\t"
    "s_barrier\n\t"
    "s_waitcnt lgkmcnt(10)\n\t"
    "v_mfma_f32_16x16x32_bf16 v[64:67], v[32:35], v[216:219], v[64:67]\n\t"
    "ds_read_b128 v[24:27], v250\n\t"
    "ds_read_b128 v[28:31], v250 offset:2048\n\t"
    "s_add_u32 m0, s16, 0x4000\n\t"
    "s_mov_b32 s4, 0x200000\n\t"
    "buffer_load_dwordx4 v199, s[52:55], s4 offen sc1 lds\n\t"
    "v_mfma_f32_16x16x32_bf16 v[68:71], v[32:35], v[220:223], v[68:71]\n\t"
    "ds_read_b64_tr_b16 v[32:33], v12 offset:29184\n\t"
    "ds_read_b64_tr_b16 v[34:35], v12 offset:32768\n\t"
    "s_waitcnt lgkmcnt(12)\n\t"
    "v_mfma_f32_16x16x32_bf16 v[72:75], v[36:39], v[216:219], v[72:75]\n\t"
    "v_mfma_f32_16x16x32_bf16 v[76:79], v[36:39], v[220:223], v[76:79]\n\t"
    "ds_read_b64_tr_b16 v[36:37], v12 offset:36480\n\t"
    "ds_read_b64_tr_b16 v[38:39], v12 offset:40064\n\t"
    "s_waitcnt lgkmcnt(12)\n\t"
    "v_mfma_f32_16x16x32_bf16 v[80:83], v[40:43], v[216:219], v[80:83]\n\t"
    "s_mul_i32 s4, s76, 0x38000\n\t"
    "s_add_u32 s4, s4, 0x1c000\n\t"
    "s_add_u32 m0, s35, 0x14000\n\t"
    "s_mov_b64 exec, s[72:73]\n\t"
    "buffer_load_dwordx4 v15, s[44:47], s4 offen lds\n\t"
    "s_mov_b64 exec, -1\n\t"
    "v_mfma_f32_16x16x32_bf16 v[84:87], v[40:43], v[220:223], v[84:87]\n\t"
    "ds_read_b64_tr_b16 v[40:41], v12 offset:29216\n\t"
    "ds_read_b64_tr_b16 v[42:43], v12 offset:32800\n\t"
    "s_waitcnt lgkmcnt(12)\n\t"
    "v_mfma_f32_16x16x32_bf16 v[88:91], v[44:47], v[216:219], v[88:91]\n\t"
    "s_add_u32 m0, s16, 0x4000\n\t"
    "s_mov_b32 s4, 0x200000\n\t"
    "buffer_load_dwordx4 v199, s[52:55], s4 offen offset:1024 sc1 lds\n\t"
    "v_mfma_f32_16x16x32_bf16 v[92:95], v[44:47], v[220:223], v[92:95]\n\t"
    "ds_read_b64_tr_b16 v[44:45], v12 offset:36512\n\t"
    "ds_read_b64_tr_b16 v[46:47], v12 offset:40096\n\t"
    "s_waitcnt lgkmcnt(12)\n\t"
    "v_mfma_f32_16x16x32_bf16 v[96:99], v[48:51], v[216:219], v[96:99]\n\t"
    "s_mul_i32 s4, s76, 0x38000\n\t"
    "s_add_u32 s4, s4, 0x1c000\n\t"
    "s_add_u32 m0, s35, 0x15c80\n\t"
    "s_mov_b64 exec, s[72:73]\n\t"
    "buffer_load_dwordx4 v15, s[48:51], s4 offen lds\n\t"
    "s_mov_b64 exec, -1\n\t"
    "v_mfma_f32_16x16x32_bf16 v[100:103], v[48:51], v[220:223], v[100:103]\n\t"
    "ds_read_b64_tr_b16 v[48:49], v12 offset:29248\n\t"
    "ds_read_b64_tr_b16 v[50:51], v12 offset:32832\n\t"
    "s_waitcnt lgkmcnt(12)\n\t"
    "v_mfma_f32_16x16x32_bf16 v[104:107], v[52:55], v[216:219], v[104:107]\n\t"
    "s_add_u32 m0, s16, 0x8000\n\t"
    "s_mov_b32 s4, 0x400000\n\t"
    "buffer_load_dwordx4 v199, s[52:55], s4 offen sc1 lds\n\t"
    "v_mfma_f32_16x16x32_bf16 v[108:111], v[52:55], v[220:223], v[108:111]\n\t"
    "ds_read_b64_tr_b16 v[52:53], v12 offset:36544\n\t"
    "ds_read_b64_tr_b16 v[54:55], v12 offset:40128\n\t"
    "s_barrier\n\t"
    "s_waitcnt lgkmcnt(10)\n\t"
    "v_mfma_f32_16x16x32_bf16 v[64:67], v[32:35], v[24:27], v[64:67]\n\t"
    "ds_read_b128 v[216:219], v251\n\t"
    "ds_read_b128 v[220:223], v251 offset:2048\n\t"
    "s_add_u32 m0, s16, 0x8000\n\t"
    "s_mov_b32 s4, 0x400000\n\t"
    "buffer_load_dwordx4 v199, s[52:55], s4 offen offset:1024 sc1 lds\n\t"
    "v_mfma_f32_16x16x32_bf16 v[68:71], v[32:35], v[28:31], v[68:71]\n\t"
    "ds_read_b64_tr_b16 v[32:33], v255\n\t"
    "ds_read_b64_tr_b16 v[34:35], v255 offset:3584\n\t"
    "s_waitcnt lgkmcnt(12)\n\t"
    "v_mfma_f32_16x16x32_bf16 v[72:75], v[36:39], v[24:27], v[72:75]\n\t"
    "v_mfma_f32_16x16x32_bf16 v[76:79], v[36:39], v[28:31], v[76:79]\n\t"
    "ds_read_b64_tr_b16 v[36:37], v255 offset:7296\n\t"
    "ds_read_b64_tr_b16 v[38:39], v255 offset:10880\n\t"
    "s_waitcnt lgkmcnt(12)\n\t"
    "v_mfma_f32_16x16x32_bf16 v[80:83], v[40:43], v[24:27], v[80:83]\n\t"
    "v_mfma_f32_16x16x32_bf16 v[84:87], v[40:43], v[28:31], v[84:87]\n\t"
    "ds_read_b64_tr_b16 v[40:41], v255 offset:32\n\t"
    "ds_read_b64_tr_b16 v[42:43], v255 offset:3616\n\t"
    "s_waitcnt lgkmcnt(12)\n\t"
    "v_mfma_f32_16x16x32_bf16 v[88:91], v[44:47], v[24:27], v[88:91]\n\t"
    "s_lshl_b32 s30, s13, 3\n\t"
    "s_add_u32 s30, s30, s8\n\t"
    "s_lshl_b32 s31, s12, 6\n\t"
    "s_add_u32 s31, s31, s30\n\t"
    "s_lshl_b32 s31, s31, 10\n\t"
    "buffer_load_dwordx4 v[192:195], v203, s[52:55], s31 offen sc1\n\t"
    "v_mfma_f32_16x16x32_bf16 v[92:95], v[44:47], v[28:31], v[92:95]\n\t"
    "ds_read_b64_tr_b16 v[44:45], v255 offset:7328\n\t"
    "ds_read_b64_tr_b16 v[46:47], v255 offset:10912\n\t"
    "s_waitcnt lgkmcnt(12)\n\t"
    "v_mfma_f32_16x16x32_bf16 v[96:99], v[48:51], v[24:27], v[96:99]\n\t"
    "v_mfma_f32_16x16x32_bf16 v[100:103], v[48:51], v[28:31], v[100:103]\n\t"
    "ds_read_b64_tr_b16 v[48:49], v255 offset:64\n\t"
    "ds_read_b64_tr_b16 v[50:51], v255 offset:3648\n\t"
    "s_waitcnt lgkmcnt(12)\n\t"
    "v_mfma_f32_16x16x32_bf16 v[104:107], v[52:55], v[24:27], v[104:107]\n\t"
    "v_mfma_f32_16x16x32_bf16 v[108:111], v[52:55], v[28:31], v[108:111]\n\t"
    "ds_read_b64_tr_b16 v[52:53], v255 offset:7360\n\t"
    "ds_read_b64_tr_b16 v[54:55], v255 offset:10944\n\t"
    "s_waitcnt vmcnt(7)\n\t"
    "s_barrier\n\t"
    "s_waitcnt lgkmcnt(10)\n\t"
    "v_mfma_f32_16x16x32_bf16 v[64:67], v[32:35], v[216:219], v[64:67]\n\t"
    "ds_read_b128 v[24:27], v248\n\t"
    "ds_read_b128 v[28:31], v248 offset:2048\n\t"
    "v_mfma_f32_16x16x32_bf16 v[68:71], v[32:35], v[220:223], v[68:71]\n\t"
    "ds_read_b64_tr_b16 v[32:33], v255 offset:14592\n\t"
    "ds_read_b64_tr_b16 v[34:35], v255 offset:18176\n\t"
    "s_waitcnt lgkmcnt(12)\n\t"
    "v_mfma_f32_16x16x32_bf16 v[72:75], v[36:39], v[216:219], v[72:75]\n\t"
    "v_mfma_f32_16x16x32_bf16 v[76:79], v[36:39], v[220:223], v[76:79]\n\t"
    "ds_read_b64_tr_b16 v[36:37], v255 offset:21888\n\t"
    "ds_read_b64_tr_b16 v[38:39], v255 offset:25472\n\t"
    "s_waitcnt lgkmcnt(12)\n\t"
    "v_mfma_f32_16x16x32_bf16 v[80:83], v[40:43], v[216:219], v[80:83]\n\t"
    "v_mfma_f32_16x16x32_bf16 v[84:87], v[40:43], v[220:223], v[84:87]\n\t"
    "ds_read_b64_tr_b16 v[40:41], v255 offset:14624\n\t"
    "ds_read_b64_tr_b16 v[42:43], v255 offset:18208\n\t"
    "s_waitcnt lgkmcnt(12)\n\t"
    "v_mfma_f32_16x16x32_bf16 v[88:91], v[44:47], v[216:219], v[88:91]\n\t"
    "v_mfma_f32_16x16x32_bf16 v[92:95], v[44:47], v[220:223], v[92:95]\n\t"
    "ds_read_b64_tr_b16 v[44:45], v255 offset:21920\n\t"
    "ds_read_b64_tr_b16 v[46:47], v255 offset:25504\n\t"
    "s_waitcnt lgkmcnt(12)\n\t"
    "v_mfma_f32_16x16x32_bf16 v[96:99], v[48:51], v[216:219], v[96:99]\n\t"
    "v_mfma_f32_16x16x32_bf16 v[100:103], v[48:51], v[220:223], v[100:103]\n\t"
    "ds_read_b64_tr_b16 v[48:49], v255 offset:14656\n\t"
    "ds_read_b64_tr_b16 v[50:51], v255 offset:18240\n\t"
    "s_waitcnt lgkmcnt(12)\n\t"
    "v_mfma_f32_16x16x32_bf16 v[104:107], v[52:55], v[216:219], v[104:107]\n\t"
    "v_mfma_f32_16x16x32_bf16 v[108:111], v[52:55], v[220:223], v[108:111]\n\t"
    "ds_read_b64_tr_b16 v[52:53], v255 offset:21952\n\t"
    "ds_read_b64_tr_b16 v[54:55], v255 offset:25536\n\t"
    "s_waitcnt vmcnt(1)\n\t"
    "s_barrier\n\t"
    "ds_read_b128 v[176:179], v204\n\t"
    "s_mov_b64 exec, s[18:19]\n\t"
    "ds_read_b128 v[180:183], v205\n\t"
    "ds_read_b128 v[184:187], v205 offset:512\n\t"
    "s_mov_b64 exec, -1\n\t"
    "s_waitcnt lgkmcnt(13)\n\t"
    "v_mfma_f32_16x16x32_bf16 v[64:67], v[32:35], v[24:27], v[64:67]\n\t"
    "ds_read_b128 v[216:219], v249\n\t"
    "ds_read_b128 v[220:223], v249 offset:2048\n\t"
    "v_mfma_f32_16x16x32_bf16 v[68:71], v[32:35], v[28:31], v[68:71]\n\t"
    "ds_read_b64_tr_b16 v[32:33], v12\n\t"
    "ds_read_b64_tr_b16 v[34:35], v12 offset:3584\n\t"
    "s_waitcnt lgkmcnt(15)\n\t"
    "v_mfma_f32_16x16x32_bf16 v[72:75], v[36:39], v[24:27], v[72:75]\n\t"
    "s_waitcnt vmcnt(0) lgkmcnt(6)\n\t"
    "v_mfma_f32_32x32x16_bf16 v[128:143], v[176:179], v[192:195], 0\n\t"
    "ds_read_b128 v[176:179], v204 offset:1024\n\t"
    "v_mfma_f32_16x16x32_bf16 v[76:79], v[36:39], v[28:31], v[76:79]\n\t"
    "ds_read_b64_tr_b16 v[36:37], v12 offset:7296\n\t"
    "ds_read_b64_tr_b16 v[38:39], v12 offset:10880\n\t"
    "v_mfma_f32_16x16x32_bf16 v[80:83], v[40:43], v[24:27], v[80:83]\n\t"
    "v_mfma_f32_16x16x32_bf16 v[84:87], v[40:43], v[28:31], v[84:87]\n\t"
    "ds_read_b64_tr_b16 v[40:41], v12 offset:32\n\t"
    "ds_read_b64_tr_b16 v[42:43], v12 offset:3616\n\t"
    "v_mfma_f32_16x16x32_bf16 v[88:91], v[44:47], v[24:27], v[88:91]\n\t"
    "v_mfma_f32_16x16x32_bf16 v[92:95], v[44:47], v[28:31], v[92:95]\n\t"
    "ds_read_b64_tr_b16 v[44:45], v12 offset:7328\n\t"
    "ds_read_b64_tr_b16 v[46:47], v12 offset:10912\n\t"
    "v_mfma_f32_16x16x32_bf16 v[96:99], v[48:51], v[24:27], v[96:99]\n\t"
    "v_mfma_f32_16x16x32_bf16 v[100:103], v[48:51], v[28:31], v[100:103]\n\t"
    "ds_read_b64_tr_b16 v[48:49], v12 offset:64\n\t"
    "ds_read_b64_tr_b16 v[50:51], v12 offset:3648\n\t"
    "v_max3_f32 v210, v128, v129, v130\n\t"
    "v_max3_f32 v211, v131, v132, v133\n\t"
    "v_max3_f32 v210, v210, v134, v135\n\t"
    "v_mfma_f32_16x16x32_bf16 v[104:107], v[52:55], v[24:27], v[104:107]\n\t"
    "v_max3_f32 v211, v211, v136, v137\n\t"
    "v_max3_f32 v210, v210, v138, v139\n\t"
    "v_max3_f32 v211, v211, v140, v141\n\t"
    "v_mfma_f32_16x16x32_bf16 v[108:111], v[52:55], v[28:31], v[108:111]\n\t"
    "ds_read_b64_tr_b16 v[52:53], v12 offset:7360\n\t"
    "ds_read_b64_tr_b16 v[54:55], v12 offset:10944\n\t"
    "s_barrier\n\t"
    "v_max3_f32 v210, v210, v142, v143\n\t"
    "v_max_f32 v210, v210, v211\n\t"
    "v_mov_b32 v211, v210\n\t"
    "s_waitcnt lgkmcnt(11)\n\t"
    "v_mfma_f32_16x16x32_bf16 v[64:67], v[32:35], v[216:219], v[64:67]\n\t"
    "v_permlane32_swap_b32 v210, v211\n\t"
    "v_max_f32 v210, v210, v211\n\t"
    "v_sub_f32 v144, 0, v210\n\t"
    "v_mfma_f32_16x16x32_bf16 v[68:71], v[32:35], v[220:223], v[68:71]\n\t"
    "v_sub_f32 v145, 0, v210\n\t"
    "v_sub_f32 v146, 0, v210\n\t"
    "v_sub_f32 v147, 0, v210\n\t"
    "s_waitcnt lgkmcnt(8)\n\t"
    "v_mfma_f32_16x16x32_bf16 v[72:75], v[36:39], v[216:219], v[72:75]\n\t"
    "v_sub_f32 v148, 0, v210\n\t"
    "v_sub_f32 v149, 0, v210\n\t"
    "v_sub_f32 v150, 0, v210\n\t"
    "v_mfma_f32_16x16x32_bf16 v[76:79], v[36:39], v[220:223], v[76:79]\n\t"
    "v_sub_f32 v151, 0, v210\n\t"
    "v_sub_f32 v152, 0, v210\n\t"
    "v_sub_f32 v153, 0, v210\n\t"
    "s_waitcnt lgkmcnt(6)\n\t"
    "v_mfma_f32_16x16x32_bf16 v[80:83], v[40:43], v[216:219], v[80:83]\n\t"
    "v_sub_f32 v154, 0, v210\n\t"
    "v_sub_f32 v155, 0, v210\n\t"
    "v_sub_f32 v156, 0, v210\n\t"
    "v_mfma_f32_16x16x32_bf16 v[84:87], v[40:43], v[220:223], v[84:87]\n\t"
    "v_sub_f32 v157, 0, v210\n\t"
    "v_sub_f32 v158, 0, v210\n\t"
    "v_sub_f32 v159, 0, v210\n\t"
    "s_waitcnt lgkmcnt(4)\n\t"
    "v_mfma_f32_16x16x32_bf16 v[88:91], v[44:47], v[216:219], v[88:91]\n\t"
    "v_sub_f32 v128, v128, v210\n\t"
    "v_sub_f32 v129, v129, v210\n\t"
    "v_sub_f32 v130, v130, v210\n\t"
    "v_mfma_f32_16x16x32_bf16 v[92:95], v[44:47], v[220:223], v[92:95]\n\t"
    "v_sub_f32 v131, v131, v210\n\t"
    "v_sub_f32 v132, v132, v210\n\t"
    "v_sub_f32 v133, v133, v210\n\t"
    "s_waitcnt lgkmcnt(2)\n\t"
    "v_mfma_f32_16x16x32_bf16 v[96:99], v[48:51], v[216:219], v[96:99]\n\t"
    "v_sub_f32 v134, v134, v210\n\t"
    "v_sub_f32 v135, v135, v210\n\t"
    "v_sub_f32 v136, v136, v210\n\t"
    "v_mfma_f32_16x16x32_bf16 v[100:103], v[48:51], v[220:223], v[100:103]\n\t"
    "v_sub_f32 v137, v137, v210\n\t"
    "v_sub_f32 v138, v138, v210\n\t"
    "v_sub_f32 v139, v139, v210\n\t"
    "s_waitcnt lgkmcnt(0)\n\t"
    "v_mfma_f32_16x16x32_bf16 v[104:107], v[52:55], v[216:219], v[104:107]\n\t"
    "v_sub_f32 v140, v140, v210\n\t"
    "v_sub_f32 v141, v141, v210\n\t"
    "v_sub_f32 v142, v142, v210\n\t"
    "v_mfma_f32_16x16x32_bf16 v[108:111], v[52:55], v[220:223], v[108:111]\n\t"
    "s_barrier\n\t"
    "v_sub_f32 v143, v143, v210\n\t"
    "s_setprio 1\n\t"
    "v_exp_f32 v128, v128\n\t"
    "v_exp_f32 v129, v129\n\t"
    "v_exp_f32 v130, v130\n\t"
    "v_exp_f32 v131, v131\n\t"
    "v_exp_f32 v132, v132\n\t"
    "v_mfma_f32_32x32x16_bf16 v[32:47], v[176:179], v[192:195], v[144:159]\n\t"
    "ds_read_b128 v[176:179], v204 offset:2048\n\t"
    "v_exp_f32 v133, v133\n\t"
    "v_mul_f32 v64, v64, v72\n\t"
    "v_mul_f32 v65, v65, v73\n\t"
    "v_mul_f32 v66, v66, v74\n\t"
    "v_mul_f32 v67, v67, v75\n\t"
    "v_mul_f32 v68, v68, v76\n\t"
    "v_mul_f32 v69, v69, v77\n\t"
    "v_mul_f32 v70, v70, v78\n\t"
    "v_mul_f32 v71, v71, v79\n\t"
    "v_cvt_pk_bf16_f32 v232, v64, v65\n\t"
    "v_cvt_pk_bf16_f32 v233, v66, v67\n\t"
    "v_cvt_pk_bf16_f32 v234, v68, v69\n\t"
    "v_cvt_pk_bf16_f32 v235, v70, v71\n\t"
    "s_nop 0\n\t"
    "v_permlane16_swap_b32 v232, v234\n\t"
    "v_permlane16_swap_b32 v233, v235\n\t"
    "buffer_store_dwordx4 v[232:235], v18, s[52:55], 0 offen sc1\n\t"
    "v_exp_f32 v134, v134\n\t"
    "v_exp_f32 v135, v135\n\t"
    "v_exp_f32 v136, v136\n\t"
    "v_mul_f32 v80, v80, v88\n\t"
    "v_mul_f32 v81, v81, v89\n\t"
    "v_mul_f32 v82, v82, v90\n\t"
    "v_mul_f32 v83, v83, v91\n\t"
    "v_mul_f32 v84, v84, v92\n\t"
    "v_mul_f32 v85, v85, v93\n\t"
    "v_mul_f32 v86, v86, v94\n\t"
    "v_mul_f32 v87, v87, v95\n\t"
    "v_cvt_pk_bf16_f32 v236, v80, v81\n\t"
    "v_cvt_pk_bf16_f32 v237, v82, v83\n\t"
    "v_cvt_pk_bf16_f32 v238, v84, v85\n\t"
    "v_cvt_pk_bf16_f32 v239, v86, v87\n\t"
    "s_nop 0\n\t"
    "v_permlane16_swap_b32 v236, v238\n\t"
    "v_permlane16_swap_b32 v237, v239\n\t"
    "buffer_store_dwordx4 v[236:239], v18, s[52:55], 0 offen offset:1024 sc1\n\t"
    "v_exp_f32 v137, v137\n\t"
    "v_smfmac_f32_32x32x32_bf16 v[160:175], v[180:183], v[128:135], v206\n\t"
    "s_mov_b64 exec, s[18:19]\n\t"
    "ds_read_b128 v[180:183], v205 offset:1024\n\t"
    "ds_read_b128 v[188:191], v205 offset:1536\n\t"
    "s_mov_b64 exec, -1\n\t"
    "v_exp_f32 v138, v138\n\t"
    "v_exp_f32 v139, v139\n\t"
    "v_mul_f32 v96, v96, v104\n\t"
    "v_mul_f32 v97, v97, v105\n\t"
    "v_mul_f32 v98, v98, v106\n\t"
    "v_mul_f32 v99, v99, v107\n\t"
    "v_mul_f32 v100, v100, v108\n\t"
    "v_mul_f32 v101, v101, v109\n\t"
    "v_mul_f32 v102, v102, v110\n\t"
    "v_mul_f32 v103, v103, v111\n\t"
    "v_cvt_pk_bf16_f32 v240, v96, v97\n\t"
    "v_cvt_pk_bf16_f32 v241, v98, v99\n\t"
    "v_cvt_pk_bf16_f32 v242, v100, v101\n\t"
    "v_cvt_pk_bf16_f32 v243, v102, v103\n\t"
    "s_nop 0\n\t"
    "v_permlane16_swap_b32 v240, v242\n\t"
    "v_permlane16_swap_b32 v241, v243\n\t"
    "buffer_store_dwordx4 v[240:243], v18, s[52:55], 0 offen offset:2048 sc1\n\t"
    "buffer_store_dwordx4 v[240:243], v18, s[52:55], 0 offen offset:2048 sc1\n\t"
    "v_exp_f32 v140, v140\n\t"
    "v_exp_f32 v141, v141\n\t"
    "v_exp_f32 v142, v142\n\t"
    "v_exp_f32 v143, v143\n\t"
    "L_join_%=:\n\t"
    "v_exp_f32 v32, v32\n\t"
    "v_exp_f32 v33, v33\n\t"
    "v_smfmac_f32_32x32x32_bf16 v[160:175], v[184:187], v[136:143], v206\n\t"
    "v_exp_f32 v34, v34\n\t"
    "v_exp_f32 v35, v35\n\t"
    "s_add_u32 m0, s16, 0xc000\n\t"
    "s_mov_b32 s4, 0x204000\n\t"
    "buffer_load_dwordx4 v199, s[52:55], s4 offen sc1 lds\n\t"
    "v_exp_f32 v36, v36\n\t"
    "s_waitcnt lgkmcnt(2)\n\t"
    "v_mfma_f32_32x32x16_bf16 v[128:143], v[176:179], v[192:195], v[144:159]\n\t"
    "ds_read_b128 v[176:179], v204 offset:3072\n\t"
    "v_exp_f32 v37, v37\n\t"
    "v_exp_f32 v38, v38\n\t"
    "v_exp_f32 v39, v39\n\t"
    "v_exp_f32 v40, v40\n\t"
    "s_add_u32 m0, s16, 0xc000\n\t"
    "s_mov_b32 s4, 0x204000\n\t"
    "buffer_load_dwordx4 v199, s[52:55], s4 offen offset:1024 sc1 lds\n\t"
    "v_exp_f32 v41, v41\n\t"
    "s_waitcnt lgkmcnt(2)\n\t"
    "v_smfmac_f32_32x32x32_bf16 v[160:175], v[180:183], v[32:39], v206\n\t"
    "s_mov_b64 exec, s[18:19]\n\t"
    "ds_read_b128 v[180:183], v205 offset:2048\n\t"
    "ds_read_b128 v[184:187], v205 offset:2560\n\t"
    "s_mov_b64 exec, -1\n\t"
    "v_exp_f32 v42, v42\n\t"
    "v_exp_f32 v43, v43\n\t"
    "v_exp_f32 v44, v44\n\t"
    "v_exp_f32 v45, v45\n\t"
    "v_exp_f32 v46, v46\n\t"
    "s_add_u32 m0, s16, 0x10000\n\t"
    "s_mov_b32 s4, 0x404000\n\t"
    "buffer_load_dwordx4 v199, s[52:55], s4 offen sc1 lds\n\t"
    "v_exp_f32 v47, v47\n\t"
    "v_exp_f32 v128, v128\n\t"
    "v_exp_f32 v129, v129\n\t"
    "s_waitcnt lgkmcnt(3)\n\t"
    "v_smfmac_f32_32x32x32_bf16 v[160:175], v[188:191], v[40:47], v206\n\t"
    "v_exp_f32 v130, v130\n\t"
    "v_exp_f32 v131, v131\n\t"
    "s_add_u32 m0, s16, 0x10000\n\t"
    "s_mov_b32 s4, 0x404000\n\t"
    "buffer_load_dwordx4 v199, s[52:55], s4 offen offset:1024 sc1 lds\n\t"
    "v_exp_f32 v132, v132\n\t"
    "s_waitcnt lgkmcnt(2)\n\t"
    "v_mfma_f32_32x32x16_bf16 v[32:47], v[176:179], v[192:195], v[144:159]\n\t"
    "ds_read_b128 v[176:179], v204 offset:4096\n\t"
    "v_exp_f32 v133, v133\n\t"
    "v_exp_f32 v134, v134\n\t"
    "v_exp_f32 v135, v135\n\t"
    "v_exp_f32 v136, v136\n\t"
    "s_add_u32 m0, s16, 0x14000\n\t"
    "s_mov_b32 s4, 0x208000\n\t"
    "buffer_load_dwordx4 v199, s[52:55], s4 offen sc1 lds\n\t"
    "v_exp_f32 v137, v137\n\t"
    "s_waitcnt lgkmcnt(2)\n\t"
    "v_smfmac_f32_32x32x32_bf16 v[160:175], v[180:183], v[128:135], v206\n\t"
    "s_mov_b64 exec, s[18:19]\n\t"
    "ds_read_b128 v[180:183], v205 offset:3072\n\t"
    "ds_read_b128 v[188:191], v205 offset:3584\n\t"
    "s_mov_b64 exec, -1\n\t"
    "v_exp_f32 v138, v138\n\t"
    "v_exp_f32 v139, v139\n\t"
    "v_exp_f32 v140, v140\n\t"
    "v_exp_f32 v141, v141\n\t"
    "v_exp_f32 v142, v142\n\t"
    "s_add_u32 m0, s16, 0x14000\n\t"
    "s_mov_b32 s4, 0x208000\n\t"
    "buffer_load_dwordx4 v199, s[52:55], s4 offen offset:1024 sc1 lds\n\t"
    "v_exp_f32 v143, v143\n\t"
    "v_exp_f32 v32, v32\n\t"
    "v_exp_f32 v33, v33\n\t"
    "s_waitcnt lgkmcnt(3)\n\t"
    "v_smfmac_f32_32x32x32_bf16 v[160:175], v[184:187], v[136:143], v206\n\t"
    "v_exp_f32 v34, v34\n\t"
    "v_exp_f32 v35, v35\n\t"
    "s_add_u32 m0, s16, 0x18000\n\t"
    "s_mov_b32 s4, 0x408000\n\t"
    "buffer_load_dwordx4 v199, s[52:55], s4 offen sc1 lds\n\t"
    "v_exp_f32 v36, v36\n\t"
    "s_waitcnt lgkmcnt(2)\n\t"
    "v_mfma_f32_32x32x16_bf16 v[128:143], v[176:179], v[192:195], v[144:159]\n\t"
    "ds_read_b128 v[176:179], v204 offset:5120\n\t"
    "v_exp_f32 v37, v37\n\t"
    "v_exp_f32 v38, v38\n\t"
    "v_exp_f32 v39, v39\n\t"
    "v_exp_f32 v40, v40\n\t"
    "s_add_u32 m0, s16, 0x18000\n\t"
    "s_mov_b32 s4, 0x408000\n\t"
    "buffer_load_dwordx4 v199, s[52:55], s4 offen offset:1024 sc1 lds\n\t"
    "v_exp_f32 v41, v41\n\t"
    "s_waitcnt lgkmcnt(2)\n\t"
    "v_smfmac_f32_32x32x32_bf16 v[160:175], v[180:183], v[32:39], v206\n\t"
    "s_mov_b64 exec, s[18:19]\n\t"
    "ds_read_b128 v[180:183], v205 offset:4096\n\t"
    "ds_read_b128 v[184:187], v205 offset:4608\n\t"
    "s_mov_b64 exec, -1\n\t"
    "v_exp_f32 v42, v42\n\t"
    "v_exp_f32 v43, v43\n\t"
    "v_exp_f32 v44, v44\n\t"
    "v_exp_f32 v45, v45\n\t"
    "v_exp_f32 v46, v46\n\t"
    "s_add_u32 m0, s16, 0x1c000\n\t"
    "s_mov_b32 s4, 0x20c000\n\t"
    "buffer_load_dwordx4 v199, s[52:55], s4 offen sc1 lds\n\t"
    "v_exp_f32 v47, v47\n\t"
    "v_exp_f32 v128, v128\n\t"
    "v_exp_f32 v129, v129\n\t"
    "s_waitcnt lgkmcnt(3)\n\t"
    "v_smfmac_f32_32x32x32_bf16 v[160:175], v[188:191], v[40:47], v206\n\t"
    "v_exp_f32 v130, v130\n\t"
    "v_exp_f32 v131, v131\n\t"
    "s_add_u32 m0, s16, 0x1c000\n\t"
    "s_mov_b32 s4, 0x20c000\n\t"
    "buffer_load_dwordx4 v199, s[52:55], s4 offen offset:1024 sc1 lds\n\t"
    "v_exp_f32 v132, v132\n\t"
    "s_waitcnt lgkmcnt(2)\n\t"
    "v_mfma_f32_32x32x16_bf16 v[32:47], v[176:179], v[192:195], v[144:159]\n\t"
    "ds_read_b128 v[176:179], v204 offset:6144\n\t"
    "v_exp_f32 v133, v133\n\t"
    "v_exp_f32 v134, v134\n\t"
    "v_exp_f32 v135, v135\n\t"
    "v_exp_f32 v136, v136\n\t"
    "s_add_u32 m0, s16, 0x20000\n\t"
    "s_mov_b32 s4, 0x40c000\n\t"
    "buffer_load_dwordx4 v199, s[52:55], s4 offen sc1 lds\n\t"
    "v_exp_f32 v137, v137\n\t"
    "s_waitcnt lgkmcnt(2)\n\t"
    "v_smfmac_f32_32x32x32_bf16 v[160:175], v[180:183], v[128:135], v206\n\t"
    "s_mov_b64 exec, s[18:19]\n\t"
    "ds_read_b128 v[180:183], v205 offset:5120\n\t"
    "ds_read_b128 v[188:191], v205 offset:5632\n\t"
    "s_mov_b64 exec, -1\n\t"
    "v_exp_f32 v138, v138\n\t"
    "v_exp_f32 v139, v139\n\t"
    "v_exp_f32 v140, v140\n\t"
    "v_exp_f32 v141, v141\n\t"
    "v_exp_f32 v142, v142\n\t"
    "s_add_u32 m0, s16, 0x20000\n\t"
    "s_mov_b32 s4, 0x40c000\n\t"
    "buffer_load_dwordx4 v199, s[52:55], s4 offen offset:1024 sc1 lds\n\t"
    "v_exp_f32 v143, v143\n\t"
    "v_exp_f32 v32, v32\n\t"
    "v_exp_f32 v33, v33\n\t"
    "s_waitcnt lgkmcnt(3)\n\t"
    "v_smfmac_f32_32x32x32_bf16 v[160:175], v[184:187], v[136:143], v206\n\t"
    "v_exp_f32 v34, v34\n\t"
    "v_exp_f32 v35, v35\n\t"
    "v_exp_f32 v36, v36\n\t"
    "s_waitcnt lgkmcnt(2)\n\t"
    "v_mfma_f32_32x32x16_bf16 v[128:143], v[176:179], v[192:195], v[144:159]\n\t"
    "ds_read_b128 v[176:179], v204 offset:7168\n\t"
    "v_exp_f32 v37, v37\n\t"
    "v_exp_f32 v38, v38\n\t"
    "s_cmp_ge_u32 s8, 4\n\t"
    "s_cbranch_scc1 L_pr1_%=\n\t"
    "s_setprio 0\n\t"
    "L_pr1_%=:\n\t"
    "v_exp_f32 v39, v39\n\t"
    "v_exp_f32 v40, v40\n\t"
    "v_exp_f32 v41, v41\n\t"
    "s_waitcnt lgkmcnt(2)\n\t"
    "v_smfmac_f32_32x32x32_bf16 v[160:175], v[180:183], v[32:39], v206\n\t"
    "s_mov_b64 exec, s[18:19]\n\t"
    "ds_read_b128 v[180:183], v205 offset:6144\n\t"
    "ds_read_b128 v[184:187], v205 offset:6656\n\t"
    "s_mov_b64 exec, -1\n\t"
    "v_exp_f32 v42, v42\n\t"
    "v_exp_f32 v43, v43\n\t"
    "v_exp_f32 v44, v44\n\t"
    "v_exp_f32 v45, v45\n\t"
    "v_exp_f32 v46, v46\n\t"
    "v_exp_f32 v47, v47\n\t"
    "v_exp_f32 v128, v128\n\t"
    "v_exp_f32 v129, v129\n\t"
    "s_waitcnt lgkmcnt(3)\n\t"
    "v_smfmac_f32_32x32x32_bf16 v[160:175], v[188:191], v[40:47], v206\n\t"
    "v_exp_f32 v130, v130\n\t"
    "v_exp_f32 v131, v131\n\t"
    "v_exp_f32 v132, v132\n\t"
    "s_waitcnt lgkmcnt(2)\n\t"
    "v_mfma_f32_32x32x16_bf16 v[32:47], v[176:179], v[192:195], v[144:159]\n\t"
    "ds_read_b128 v[176:179], v204 offset:8192\n\t"
    "v_exp_f32 v133, v133\n\t"
    "v_exp_f32 v134, v134\n\t"
    "v_exp_f32 v135, v135\n\t"
    "v_exp_f32 v136, v136\n\t"
    "v_exp_f32 v137, v137\n\t"
    "s_waitcnt lgkmcnt(2)\n\t"
    "v_smfmac_f32_32x32x32_bf16 v[160:175], v[180:183], v[128:135], v206\n\t"
    "s_mov_b64 exec, s[18:19]\n\t"
    "ds_read_b128 v[180:183], v205 offset:7168\n\t"
    "ds_read_b128 v[188:191], v205 offset:7680\n\t"
    "s_mov_b64 exec, -1\n\t"
    "v_exp_f32 v138, v138\n\t"
    "v_exp_f32 v139, v139\n\t"
    "v_exp_f32 v140, v140\n\t"
    "v_exp_f32 v141, v141\n\t"
    "v_exp_f32 v142, v142\n\t"
    "v_exp_f32 v143, v143\n\t"
    "v_exp_f32 v32, v32\n\t"
    "v_exp_f32 v33, v33\n\t"
    "s_waitcnt lgkmcnt(3)\n\t"
    "v_smfmac_f32_32x32x32_bf16 v[160:175], v[184:187], v[136:143], v206\n\t"
    "v_exp_f32 v34, v34\n\t"
    "v_exp_f32 v35, v35\n\t"
    "v_exp_f32 v36, v36\n\t"
    "s_waitcnt lgkmcnt(2)\n\t"
    "v_mfma_f32_32x32x16_bf16 v[128:143], v[176:179], v[192:195], v[144:159]\n\t"
    "ds_read_b128 v[176:179], v204 offset:9216\n\t"
    "v_exp_f32 v37, v37\n\t"
    "v_exp_f32 v38, v38\n\t"
    "s_waitcnt vmcnt(0)\n\t"
    "s_barrier\n\t"
    "s_lshl_b32 s30, s9, 4\n\t"
    "s_add_u32 s30, s30, s10\n\t"
    "s_lshl_b32 s30, s30, 3\n\t"
    "s_add_u32 s30, s30, 0x1004800\n\t"
    "s_cmp_eq_u32 s8, 0\n\t"
    "s_cselect_b64 s[24:25], 1, 0\n\t"
    "v_mov_b32 v212, s82\n\t"
    "v_mov_b32 v213, s83\n\t"
    "v_mov_b32 v214, s30\n\t"
    "s_mov_b64 exec, s[24:25]\n\t"
    "buffer_store_dwordx2 v[212:213], v214, s[52:55], 0 offen sc1\n\t"
    "s_mov_b64 exec, -1\n\t"
    "s_cmp_ge_u32 s8, 4\n\t"
    "s_cbranch_scc1 L_pr2_%=\n\t"
    "s_setprio 2\n\t"
    "L_pr2_%=:\n\t"
    "v_exp_f32 v39, v39\n\t"
    "v_exp_f32 v40, v40\n\t"
    "v_exp_f32 v41, v41\n\t"
    "s_waitcnt lgkmcnt(2)\n\t"
    "v_smfmac_f32_32x32x32_bf16 v[160:175], v[180:183], v[32:39], v206\n\t"
    "s_mov_b64 exec, s[18:19]\n\t"
    "ds_read_b128 v[180:183], v205 offset:8192\n\t"
    "ds_read_b128 v[184:187], v205 offset:8704\n\t"
    "s_mov_b64 exec, -1\n\t"
    "v_exp_f32 v42, v42\n\t"
    "v_exp_f32 v43, v43\n\t"
    "v_exp_f32 v44, v44\n\t"
    "v_exp_f32 v45, v45\n\t"
    "v_exp_f32 v46, v46\n\t"
    "v_exp_f32 v47, v47\n\t"
    "v_exp_f32 v128, v128\n\t"
    "v_exp_f32 v129, v129\n\t"
    "s_waitcnt lgkmcnt(3)\n\t"
    "v_smfmac_f32_32x32x32_bf16 v[160:175], v[188:191], v[40:47], v206\n\t"
    "v_exp_f32 v130, v130\n\t"
    "v_exp_f32 v131, v131\n\t"
    "v_exp_f32 v132, v132\n\t"
    "s_waitcnt lgkmcnt(2)\n\t"
    "v_mfma_f32_32x32x16_bf16 v[32:47], v[176:179], v[192:195], v[144:159]\n\t"
    "ds_read_b128 v[176:179], v204 offset:10240\n\t"
    "v_exp_f32 v133, v133\n\t"
    "v_exp_f32 v134, v134\n\t"
    "v_exp_f32 v135, v135\n\t"
    "s_lshl_b32 s30, %1, 3\n\t"
    "s_add_u32 s30, s30, s8\n\t"
    "v_lshlrev_b32 v251, 7, v1\n\t"
    "s_mul_i32 s31, s30, 0x380\n\t"
    "s_mov_b64 exec, 0x7f\n\t"
    "buffer_load_dword v254, v251, s[56:59], s31 offen\n\t"
    "s_mul_i32 s31, s30, 0x100\n\t"
    "s_mov_b64 exec, 3\n\t"
    "buffer_load_dword v254, v251, s[60:63], s31 offen\n\t"
    "s_mov_b64 exec, -1\n\t"
    "v_exp_f32 v136, v136\n\t"
    "v_exp_f32 v137, v137\n\t"
    "s_waitcnt lgkmcnt(2)\n\t"
    "v_smfmac_f32_32x32x32_bf16 v[160:175], v[180:183], v[128:135], v206\n\t"
    "s_mov_b64 exec, s[18:19]\n\t"
    "ds_read_b128 v[180:183], v205 offset:9216\n\t"
    "ds_read_b128 v[188:191], v205 offset:9728\n\t"
    "s_mov_b64 exec, -1\n\t"
    "v_exp_f32 v138, v138\n\t"
    "v_exp_f32 v139, v139\n\t"
    "v_exp_f32 v140, v140\n\t"
    "v_exp_f32 v141, v141\n\t"
    "v_exp_f32 v142, v142\n\t"
    "v_exp_f32 v143, v143\n\t"
    "v_exp_f32 v32, v32\n\t"
    "v_exp_f32 v33, v33\n\t"
    "s_waitcnt lgkmcnt(3)\n\t"
    "v_smfmac_f32_32x32x32_bf16 v[160:175], v[184:187], v[136:143], v206\n\t"
    "v_exp_f32 v34, v34\n\t"
    "v_exp_f32 v35, v35\n\t"
    "v_exp_f32 v36, v36\n\t"
    "s_waitcnt lgkmcnt(2)\n\t"
    "v_mfma_f32_32x32x16_bf16 v[128:143], v[176:179], v[192:195], v[144:159]\n\t"
    "ds_read_b128 v[176:179], v204 offset:11264\n\t"
    "v_exp_f32 v37, v37\n\t"
    "v_exp_f32 v38, v38\n\t"
    "v_exp_f32 v39, v39\n\t"
    "v_exp_f32 v40, v40\n\t"
    "v_exp_f32 v41, v41\n\t"
    "s_waitcnt lgkmcnt(2)\n\t"
    "v_smfmac_f32_32x32x32_bf16 v[160:175], v[180:183], v[32:39], v206\n\t"
    "s_mov_b64 exec, s[18:19]\n\t"
    "ds_read_b128 v[180:183], v205 offset:10240\n\t"
    "ds_read_b128 v[184:187], v205 offset:10752\n\t"
    "s_mov_b64 exec, -1\n\t"
    "v_exp_f32 v42, v42\n\t"
    "v_exp_f32 v43, v43\n\t"
    "v_exp_f32 v44, v44\n\t"
    "v_exp_f32 v45, v45\n\t"
    "v_exp_f32 v46, v46\n\t"
    "v_exp_f32 v47, v47\n\t"
    "v_exp_f32 v128, v128\n\t"
    "v_exp_f32 v129, v129\n\t"
    "s_waitcnt lgkmcnt(3)\n\t"
    "v_smfmac_f32_32x32x32_bf16 v[160:175], v[188:191], v[40:47], v206\n\t"
    "v_exp_f32 v130, v130\n\t"
    "v_exp_f32 v131, v131\n\t"
    "v_exp_f32 v132, v132\n\t"
    "s_waitcnt lgkmcnt(2)\n\t"
    "v_mfma_f32_32x32x16_bf16 v[32:47], v[176:179], v[192:195], v[144:159]\n\t"
    "ds_read_b128 v[176:179], v204 offset:12288\n\t"
    "v_exp_f32 v133, v133\n\t"
    "v_exp_f32 v134, v134\n\t"
    "v_exp_f32 v135, v135\n\t"
    "v_exp_f32 v136, v136\n\t"
    "v_exp_f32 v137, v137\n\t"
    "s_waitcnt lgkmcnt(2)\n\t"
    "v_smfmac_f32_32x32x32_bf16 v[160:175], v[180:183], v[128:135], v206\n\t"
    "s_mov_b64 exec, s[18:19]\n\t"
    "ds_read_b128 v[180:183], v205 offset:11264\n\t"
    "ds_read_b128 v[188:191], v205 offset:11776\n\t"
    "s_mov_b64 exec, -1\n\t"
    "v_exp_f32 v138, v138\n\t"
    "v_exp_f32 v139, v139\n\t"
    "v_exp_f32 v140, v140\n\t"
    "v_exp_f32 v141, v141\n\t"
    "v_exp_f32 v142, v142\n\t"
    "v_exp_f32 v143, v143\n\t"
    "v_exp_f32 v32, v32\n\t"
    "v_exp_f32 v33, v33\n\t"
    "s_waitcnt lgkmcnt(3)\n\t"
    "v_smfmac_f32_32x32x32_bf16 v[160:175], v[184:187], v[136:143], v206\n\t"
    "v_exp_f32 v34, v34\n\t"
    "v_exp_f32 v35, v35\n\t"
    "v_exp_f32 v36, v36\n\t"
    "s_waitcnt lgkmcnt(2)\n\t"
    "v_mfma_f32_32x32x16_bf16 v[128:143], v[176:179], v[192:195], v[144:159]\n\t"
    "ds_read_b128 v[176:179], v204 offset:13312\n\t"
    "v_exp_f32 v37, v37\n\t"
    "v_exp_f32 v38, v38\n\t"
    "v_exp_f32 v39, v39\n\t"
    "v_exp_f32 v40, v40\n\t"
    "v_exp_f32 v41, v41\n\t"
    "s_waitcnt lgkmcnt(2)\n\t"
    "v_smfmac_f32_32x32x32_bf16 v[160:175], v[180:183], v[32:39], v206\n\t"
    "s_mov_b64 exec, s[18:19]\n\t"
    "ds_read_b128 v[180:183], v205 offset:12288\n\t"
    "ds_read_b128 v[184:187], v205 offset:12800\n\t"
    "s_mov_b64 exec, -1\n\t"
    "v_exp_f32 v42, v42\n\t"
    "v_exp_f32 v43, v43\n\t"
    "v_exp_f32 v44, v44\n\t"
    "v_exp_f32 v45, v45\n\t"
    "v_exp_f32 v46, v46\n\t"
    "s_add_u32 m0, s15, 0\n\t"
    "s_mov_b32 s4, 0\n\t"
    "buffer_load_dwordx4 v207, s[56:59], s4 offen lds\n\t"
    "v_exp_f32 v47, v47\n\t"
    "v_exp_f32 v128, v128\n\t"
    "v_exp_f32 v129, v129\n\t"
    "s_waitcnt lgkmcnt(3)\n\t"
    "v_smfmac_f32_32x32x32_bf16 v[160:175], v[188:191], v[40:47], v206\n\t"
    "v_exp_f32 v130, v130\n\t"
    "v_exp_f32 v131, v131\n\t"
    "v_exp_f32 v132, v132\n\t"
    "s_waitcnt lgkmcnt(2)\n\t"
    "v_mfma_f32_32x32x16_bf16 v[32:47], v[176:179], v[192:195], v[144:159]\n\t"
    "ds_read_b128 v[176:179], v204 offset:14336\n\t"
    "v_exp_f32 v133, v133\n\t"
    "v_exp_f32 v134, v134\n\t"
    "v_exp_f32 v135, v135\n\t"
    "v_exp_f32 v136, v136\n\t"
    "v_exp_f32 v137, v137\n\t"
    "s_waitcnt lgkmcnt(2)\n\t"
    "v_smfmac_f32_32x32x32_bf16 v[160:175], v[180:183], v[128:135], v206\n\t"
    "s_mov_b64 exec, s[18:19]\n\t"
    "ds_read_b128 v[180:183], v205 offset:13312\n\t"
    "ds_read_b128 v[188:191], v205 offset:13824\n\t"
    "s_mov_b64 exec, -1\n\t"
    "v_exp_f32 v138, v138\n\t"
    "v_exp_f32 v139, v139\n\t"
    "v_exp_f32 v140, v140\n\t"
    "v_exp_f32 v141, v141\n\t"
    "v_exp_f32 v142, v142\n\t"
    "s_add_u32 m0, s15, 0x400\n\t"
    "s_mov_b32 s4, 0x2000\n\t"
    "buffer_load_dwordx4 v207, s[56:59], s4 offen lds\n\t"
    "v_exp_f32 v143, v143\n\t"
    "v_exp_f32 v32, v32\n\t"
    "v_exp_f32 v33, v33\n\t"
    "s_waitcnt lgkmcnt(3)\n\t"
    "v_smfmac_f32_32x32x32_bf16 v[160:175], v[184:187], v[136:143], v206\n\t"
    "v_exp_f32 v34, v34\n\t"
    "v_exp_f32 v35, v35\n\t"
    "v_exp_f32 v36, v36\n\t"
    "s_waitcnt lgkmcnt(2)\n\t"
    "v_mfma_f32_32x32x16_bf16 v[128:143], v[176:179], v[192:195], v[144:159]\n\t"
    "ds_read_b128 v[176:179], v204 offset:15360\n\t"
    "v_exp_f32 v37, v37\n\t"
    "v_exp_f32 v38, v38\n\t"
    "v_exp_f32 v39, v39\n\t"
    "v_exp_f32 v40, v40\n\t"
    "v_exp_f32 v41, v41\n\t"
    "s_waitcnt lgkmcnt(2)\n\t"
    "v_smfmac_f32_32x32x32_bf16 v[160:175], v[180:183], v[32:39], v206\n\t"
    "s_mov_b64 exec, s[18:19]\n\t"
    "ds_read_b128 v[180:183], v205 offset:14336\n\t"
    "ds_read_b128 v[184:187], v205 offset:14848\n\t"
    "s_mov_b64 exec, -1\n\t"
    "v_exp_f32 v42, v42\n\t"
    "v_exp_f32 v43, v43\n\t"
    "v_exp_f32 v44, v44\n\t"
    "v_exp_f32 v45, v45\n\t"
    "v_exp_f32 v46, v46\n\t"
    "s_add_u32 m0, s15, 0x800\n\t"
    "s_mov_b32 s4, 0x20000\n\t"
    "buffer_load_dwordx4 v207, s[56:59], s4 offen lds\n\t"
    "v_exp_f32 v47, v47\n\t"
    "v_exp_f32 v128, v128\n\t"
    "v_exp_f32 v129, v129\n\t"
    "s_waitcnt lgkmcnt(3)\n\t"
    "v_smfmac_f32_32x32x32_bf16 v[160:175], v[188:191], v[40:47], v206\n\t"
    "v_exp_f32 v130, v130\n\t"
    "v_exp_f32 v131, v131\n\t"
    "v_exp_f32 v132, v132\n\t"
    "s_waitcnt lgkmcnt(2)\n\t"
    "v_mfma_f32_32x32x16_bf16 v[32:47], v[176:179], v[192:195], v[144:159]\n\t"
    "ds_read_b128 v[176:179], v204 offset:32768\n\t"
    "v_exp_f32 v133, v133\n\t"
    "v_exp_f32 v134, v134\n\t"
    "s_lshl_b32 s30, s9, 7\n\t"
    "s_add_u32 s30, s30, 0x1004800\n\t"
    "v_and_b32 v202, 15, v1\n\t"
    "v_lshlrev_b32 v202, 3, v202\n\t"
    "v_add_u32 v202, s30, v202\n\t"
    "buffer_load_dwordx2 v[200:201], v202, s[52:55], 0 offen sc1\n\t"
    "v_exp_f32 v135, v135\n\t"
    "v_exp_f32 v136, v136\n\t"
    "v_exp_f32 v137, v137\n\t"
    "s_waitcnt lgkmcnt(2)\n\t"
    "v_smfmac_f32_32x32x32_bf16 v[160:175], v[180:183], v[128:135], v206\n\t"
    "s_mov_b64 exec, s[18:19]\n\t"
    "ds_read_b128 v[180:183], v205 offset:15360\n\t"
    "ds_read_b128 v[188:191], v205 offset:15872\n\t"
    "s_mov_b64 exec, -1\n\t"
    "v_exp_f32 v138, v138\n\t"
    "v_exp_f32 v139, v139\n\t"
    "v_exp_f32 v140, v140\n\t"
    "v_exp_f32 v141, v141\n\t"
    "v_exp_f32 v142, v142\n\t"
    "s_add_u32 m0, s15, 0xc00\n\t"
    "s_mov_b32 s4, 0x22000\n\t"
    "buffer_load_dwordx4 v207, s[56:59], s4 offen lds\n\t"
    "v_exp_f32 v143, v143\n\t"
    "v_exp_f32 v32, v32\n\t"
    "v_exp_f32 v33, v33\n\t"
    "s_waitcnt lgkmcnt(3)\n\t"
    "v_smfmac_f32_32x32x32_bf16 v[160:175], v[184:187], v[136:143], v206\n\t"
    "v_exp_f32 v34, v34\n\t"
    "v_exp_f32 v35, v35\n\t"
    "v_exp_f32 v36, v36\n\t"
    "s_waitcnt lgkmcnt(2)\n\t"
    "v_mfma_f32_32x32x16_bf16 v[128:143], v[176:179], v[192:195], v[144:159]\n\t"
    "ds_read_b128 v[176:179], v204 offset:33792\n\t"
    "v_exp_f32 v37, v37\n\t"
    "v_exp_f32 v38, v38\n\t"
    "v_exp_f32 v39, v39\n\t"
    "v_exp_f32 v40, v40\n\t"
    "v_exp_f32 v41, v41\n\t"
    "s_waitcnt lgkmcnt(2)\n\t"
    "v_smfmac_f32_32x32x32_bf16 v[160:175], v[180:183], v[32:39], v206\n\t"
    "s_mov_b64 exec, s[18:19]\n\t"
    "ds_read_b128 v[180:183], v205 offset:32768\n\t"
    "ds_read_b128 v[184:187], v205 offset:33280\n\t"
    "s_mov_b64 exec, -1\n\t"
    "v_exp_f32 v42, v42\n\t"
    "v_exp_f32 v43, v43\n\t"
    "v_exp_f32 v44, v44\n\t"
    "v_exp_f32 v45, v45\n\t"
    "v_exp_f32 v46, v46\n\t"
    "v_exp_f32 v47, v47\n\t"
    "v_exp_f32 v128, v128\n\t"
    "v_exp_f32 v129, v129\n\t"
    "s_waitcnt lgkmcnt(3)\n\t"
    "v_smfmac_f32_32x32x32_bf16 v[160:175], v[188:191], v[40:47], v206\n\t"
    "v_exp_f32 v130, v130\n\t"
    "v_exp_f32 v131, v131\n\t"
    "v_exp_f32 v132, v132\n\t"
    "s_waitcnt lgkmcnt(2)\n\t"
    "v_mfma_f32_32x32x16_bf16 v[32:47], v[176:179], v[192:195], v[144:159]\n\t"
    "ds_read_b128 v[176:179], v204 offset:34816\n\t"
    "v_exp_f32 v133, v133\n\t"
    "v_exp_f32 v134, v134\n\t"
    "v_exp_f32 v135, v135\n\t"
    "v_exp_f32 v136, v136\n\t"
    "v_exp_f32 v137, v137\n\t"
    "s_waitcnt lgkmcnt(2)\n\t"
    "v_smfmac_f32_32x32x32_bf16 v[160:175], v[180:183], v[128:135], v206\n\t"
    "s_mov_b64 exec, s[18:19]\n\t"
    "ds_read_b128 v[180:183], v205 offset:33792\n\t"
    "ds_read_b128 v[188:191], v205 offset:34304\n\t"
    "s_mov_b64 exec, -1\n\t"
    "v_exp_f32 v138, v138\n\t"
    "v_exp_f32 v139, v139\n\t"
    "v_exp_f32 v140, v140\n\t"
    "v_exp_f32 v141, v141\n\t"
    "v_exp_f32 v142, v142\n\t"
    "v_exp_f32 v143, v143\n\t"
    "v_exp_f32 v32, v32\n\t"
    "v_exp_f32 v33, v33\n\t"
    "s_waitcnt lgkmcnt(3)\n\t"
    "v_smfmac_f32_32x32x32_bf16 v[160:175], v[184:187], v[136:143], v206\n\t"
    "v_exp_f32 v34, v34\n\t"
    "v_exp_f32 v35, v35\n\t"
    "v_exp_f32 v36, v36\n\t"
    "s_waitcnt lgkmcnt(2)\n\t"
    "v_mfma_f32_32x32x16_bf16 v[128:143], v[176:179], v[192:195], v[144:159]\n\t"
    "ds_read_b128 v[176:179], v204 offset:35840\n\t"
    "v_exp_f32 v37, v37\n\t"
    "v_exp_f32 v38, v38\n\t"
    "s_waitcnt vmcnt(1)\n\t"
    "v_cmp_ne_u64 vcc, s[82:83], v[200:201]\n\t"
    "s_nop 1\n\t"
    "s_cbranch_vccnz L_s2_retry_%=\n\t"
    "L_s2_ok_%=:\n\t"
    "s_mov_b32 s11, 0\n\t"
    "buffer_load_dwordx4 v[48:51], v20, s[52:55], s11 offen sc1\n\t"
    "s_mov_b32 s11, 0x24000\n\t"
    "buffer_load_dwordx4 v[52:55], v20, s[52:55], s11 offen sc1\n\t"
    "v_exp_f32 v39, v39\n\t"
    "v_exp_f32 v40, v40\n\t"
    "v_exp_f32 v41, v41\n\t"
    "s_waitcnt lgkmcnt(2)\n\t"
    "v_smfmac_f32_32x32x32_bf16 v[160:175], v[180:183], v[32:39], v206\n\t"
    "s_mov_b64 exec, s[18:19]\n\t"
    "ds_read_b128 v[180:183], v205 offset:34816\n\t"
    "ds_read_b128 v[184:187], v205 offset:35328\n\t"
    "s_mov_b64 exec, -1\n\t"
    "v_exp_f32 v42, v42\n\t"
    "v_exp_f32 v43, v43\n\t"
    "v_exp_f32 v44, v44\n\t"
    "v_exp_f32 v45, v45\n\t"
    "v_exp_f32 v46, v46\n\t"
    "v_exp_f32 v47, v47\n\t"
    "v_exp_f32 v128, v128\n\t"
    "v_exp_f32 v129, v129\n\t"
    "s_waitcnt lgkmcnt(3)\n\t"
    "v_smfmac_f32_32x32x32_bf16 v[160:175], v[188:191], v[40:47], v206\n\t"
    "v_exp_f32 v130, v130\n\t"
    "v_exp_f32 v131, v131\n\t"
    "v_exp_f32 v132, v132\n\t"
    "s_waitcnt lgkmcnt(2)\n\t"
    "v_mfma_f32_32x32x16_bf16 v[32:47], v[176:179], v[192:195], v[144:159]\n\t"
    "ds_read_b128 v[176:179], v204 offset:36864\n\t"
    "v_exp_f32 v133, v133\n\t"
    "v_exp_f32 v134, v134\n\t"
    "s_mov_b32 s11, 0x2000\n\t"
    "buffer_load_dwordx4 v[56:59], v20, s[52:55], s11 offen sc1\n\t"
    "v_exp_f32 v135, v135\n\t"
    "v_exp_f32 v136, v136\n\t"
    "v_exp_f32 v137, v137\n\t"
    "s_waitcnt lgkmcnt(2)\n\t"
    "v_smfmac_f32_32x32x32_bf16 v[160:175], v[180:183], v[128:135], v206\n\t"
    "s_mov_b64 exec, s[18:19]\n\t"
    "ds_read_b128 v[180:183], v205 offset:35840\n\t"
    "ds_read_b128 v[188:191], v205 offset:36352\n\t"
    "s_mov_b64 exec, -1\n\t"
    "v_exp_f32 v138, v138\n\t"
    "v_exp_f32 v139, v139\n\t"
    "v_exp_f32 v140, v140\n\t"
    "v_exp_f32 v141, v141\n\t"
    "v_exp_f32 v142, v142\n\t"
    "s_mov_b32 s11, 0x26000\n\t"
    "buffer_load_dwordx4 v[60:63], v20, s[52:55], s11 offen sc1\n\t"
    "v_exp_f32 v143, v143\n\t"
    "v_exp_f32 v32, v32\n\t"
    "v_exp_f32 v33, v33\n\t"
    "s_waitcnt lgkmcnt(3)\n\t"
    "v_smfmac_f32_32x32x32_bf16 v[160:175], v[184:187], v[136:143], v206\n\t"
    "v_exp_f32 v34, v34\n\t"
    "v_exp_f32 v35, v35\n\t"
    "v_exp_f32 v36, v36\n\t"
    "s_waitcnt lgkmcnt(2)\n\t"
    "v_mfma_f32_32x32x16_bf16 v[128:143], v[176:179], v[192:195], v[144:159]\n\t"
    "ds_read_b128 v[176:179], v204 offset:37888\n\t"
    "v_exp_f32 v37, v37\n\t"
    "v_exp_f32 v38, v38\n\t"
    "v_exp_f32 v39, v39\n\t"
    "v_exp_f32 v40, v40\n\t"
    "v_exp_f32 v41, v41\n\t"
    "s_waitcnt lgkmcnt(2)\n\t"
    "v_smfmac_f32_32x32x32_bf16 v[160:175], v[180:183], v[32:39], v206\n\t"
    "s_mov_b64 exec, s[18:19]\n\t"
    "ds_read_b128 v[180:183], v205 offset:36864\n\t"
    "ds_read_b128 v[184:187], v205 offset:37376\n\t"
    "s_mov_b64 exec, -1\n\t"
    "v_exp_f32 v42, v42\n\t"
    "v_exp_f32 v43, v43\n\t"
    "v_exp_f32 v44, v44\n\t"
    "v_exp_f32 v45, v45\n\t"
    "v_exp_f32 v46, v46\n\t"
    "v_exp_f32 v47, v47\n\t"
    "v_exp_f32 v128, v128\n\t"
    "v_exp_f32 v129, v129\n\t"
    "s_waitcnt lgkmcnt(3)\n\t"
    "v_smfmac_f32_32x32x32_bf16 v[160:175], v[188:191], v[40:47], v206\n\t"
    "v_exp_f32 v130, v130\n\t"
    "v_exp_f32 v131, v131\n\t"
    "v_exp_f32 v132, v132\n\t"
    "s_waitcnt lgkmcnt(2)\n\t"
    "v_mfma_f32_32x32x16_bf16 v[32:47], v[176:179], v[192:195], v[144:159]\n\t"
    "ds_read_b128 v[176:179], v204 offset:38912\n\t"
    "v_exp_f32 v133, v133\n\t"
    "v_exp_f32 v134, v134\n\t"
    "v_exp_f32 v135, v135\n\t"
    "v_exp_f32 v136, v136\n\t"
    "v_exp_f32 v137, v137\n\t"
    "s_waitcnt lgkmcnt(2)\n\t"
    "v_smfmac_f32_32x32x32_bf16 v[160:175], v[180:183], v[128:135], v206\n\t"
    "s_mov_b64 exec, s[18:19]\n\t"
    "ds_read_b128 v[180:183], v205 offset:37888\n\t"
    "ds_read_b128 v[188:191], v205 offset:38400\n\t"
    "s_mov_b64 exec, -1\n\t"
    "v_exp_f32 v138, v138\n\t"
    "v_exp_f32 v139, v139\n\t"
    "v_exp_f32 v140, v140\n\t"
    "v_exp_f32 v141, v141\n\t"
    "v_exp_f32 v142, v142\n\t"
    "v_exp_f32 v143, v143\n\t"
    "v_exp_f32 v32, v32\n\t"
    "v_exp_f32 v33, v33\n\t"
    "s_waitcnt lgkmcnt(3)\n\t"
    "v_smfmac_f32_32x32x32_bf16 v[160:175], v[184:187], v[136:143], v206\n\t"
    "v_exp_f32 v34, v34\n\t"
    "v_exp_f32 v35, v35\n\t"
    "v_exp_f32 v36, v36\n\t"
    "s_waitcnt lgkmcnt(2)\n\t"
    "v_mfma_f32_32x32x16_bf16 v[128:143], v[176:179], v[192:195], v[144:159]\n\t"
    "ds_read_b128 v[176:179], v204 offset:39936\n\t"
    "v_exp_f32 v37, v37\n\t"
    "v_exp_f32 v38, v38\n\t"
    "v_exp_f32 v39, v39\n\t"
    "v_exp_f32 v40, v40\n\t"
    "v_exp_f32 v41, v41\n\t"
    "s_waitcnt lgkmcnt(2)\n\t"
    "v_smfmac_f32_32x32x32_bf16 v[160:175], v[180:183], v[32:39], v206\n\t"
    "s_mov_b64 exec, s[18:19]\n\t"
    "ds_read_b128 v[180:183], v205 offset:38912\n\t"
    "ds_read_b128 v[184:187], v205 offset:39424\n\t"
    "s_mov_b64 exec, -1\n\t"
    "v_exp_f32 v42, v42\n\t"
    "v_exp_f32 v43, v43\n\t"
    "v_exp_f32 v44, v44\n\t"
    "v_exp_f32 v45, v45\n\t"
    "v_exp_f32 v46, v46\n\t"
    "v_exp_f32 v47, v47\n\t"
    "v_exp_f32 v128, v128\n\t"
    "v_exp_f32 v129, v129\n\t"
    "s_waitcnt lgkmcnt(3)\n\t"
    "v_smfmac_f32_32x32x32_bf16 v[160:175], v[188:191], v[40:47], v206\n\t"
    "v_exp_f32 v130, v130\n\t"
    "ds_read_b64_tr_b16 v[24:25], v252\n\t"
    "ds_read_b64_tr_b16 v[26:27], v252 offset:512\n\t"
    "ds_read_b64_tr_b16 v[28:29], v253\n\t"
    "ds_read_b64_tr_b16 v[30:31], v253 offset:512\n\t"
    "v_exp_f32 v131, v131\n\t"
    "v_exp_f32 v132, v132\n\t"
    "s_waitcnt lgkmcnt(6)\n\t"
    "v_mfma_f32_32x32x16_bf16 v[32:47], v[176:179], v[192:195], v[144:159]\n\t"
    "ds_read_b128 v[176:179], v204 offset:40960\n\t"
    "v_exp_f32 v133, v133\n\t"
    "s_mov_b32 s11, 0x4000\n\t"
    "buffer_load_dwordx4 v[2:5], v20, s[52:55], s11 offen sc1\n\t"
    "v_exp_f32 v134, v134\n\t"
    "v_exp_f32 v135, v135\n\t"
    "v_exp_f32 v136, v136\n\t"
    "v_exp_f32 v137, v137\n\t"
    "s_waitcnt lgkmcnt(6)\n\t"
    "v_smfmac_f32_32x32x32_bf16 v[160:175], v[180:183], v[128:135], v206\n\t"
    "s_mov_b64 exec, s[18:19]\n\t"
    "ds_read_b128 v[180:183], v205 offset:39936\n\t"
    "ds_read_b128 v[188:191], v205 offset:40448\n\t"
    "s_mov_b64 exec, -1\n\t"
    "v_exp_f32 v138, v138\n\t"
    "v_exp_f32 v139, v139\n\t"
    "v_exp_f32 v140, v140\n\t"
    "s_waitcnt vmcnt(4) lgkmcnt(5)\n\t"
    "v_mfma_f32_32x32x16_bf16 v[64:79], v[24:27], v[48:51], 0\n\t"
    "v_exp_f32 v141, v141\n\t"
    "v_exp_f32 v142, v142\n\t"
    "s_mov_b32 s11, 0x28000\n\t"
    "buffer_load_dwordx4 v[6:9], v20, s[52:55], s11 offen sc1\n\t"
    "v_exp_f32 v143, v143\n\t"
    "v_exp_f32 v32, v32\n\t"
    "v_exp_f32 v33, v33\n\t"
    "v_smfmac_f32_32x32x32_bf16 v[160:175], v[184:187], v[136:143], v206\n\t"
    "v_exp_f32 v34, v34\n\t"
    "v_exp_f32 v35, v35\n\t"
    "v_exp_f32 v36, v36\n\t"
    "s_waitcnt lgkmcnt(2)\n\t"
    "v_mfma_f32_32x32x16_bf16 v[128:143], v[176:179], v[192:195], v[144:159]\n\t"
    "ds_read_b128 v[176:179], v204 offset:41984\n\t"
    "v_exp_f32 v37, v37\n\t"
    "v_exp_f32 v38, v38\n\t"
    "v_exp_f32 v39, v39\n\t"
    "v_exp_f32 v40, v40\n\t"
    "v_exp_f32 v41, v41\n\t"
    "s_waitcnt lgkmcnt(2)\n\t"
    "v_smfmac_f32_32x32x32_bf16 v[160:175], v[180:183], v[32:39], v206\n\t"
    "s_mov_b64 exec, s[18:19]\n\t"
    "ds_read_b128 v[180:183], v205 offset:40960\n\t"
    "ds_read_b128 v[184:187], v205 offset:41472\n\t"
    "s_mov_b64 exec, -1\n\t"
    "v_exp_f32 v42, v42\n\t"
    "v_exp_f32 v43, v43\n\t"
    "v_exp_f32 v44, v44\n\t"
    "v_mfma_f32_32x32x16_bf16 v[80:95], v[28:31], v[48:51], 0\n\t"
    "v_exp_f32 v45, v45\n\t"
    "s_add_u32 m0, s15, 0\n\t"
    "s_mov_b32 s4, 0x40000\n\t"
    "buffer_load_dwordx4 v207, s[56:59], s4 offen lds\n\t"
    "v_exp_f32 v46, v46\n\t"
    "v_exp_f32 v47, v47\n\t"
    "v_exp_f32 v128, v128\n\t"
    "v_exp_f32 v129, v129\n\t"
    "s_waitcnt lgkmcnt(3)\n\t"
    "v_smfmac_f32_32x32x32_bf16 v[160:175], v[188:191], v[40:47], v206\n\t"
    "v_exp_f32 v130, v130\n\t"
    "v_exp_f32 v131, v131\n\t"
    "s_add_u32 m0, s15, 0x400\n\t"
    "s_mov_b32 s4, 0x42000\n\t"
    "buffer_load_dwordx4 v207, s[56:59], s4 offen lds\n\t"
    "v_exp_f32 v132, v132\n\t"
    "s_waitcnt lgkmcnt(2)\n\t"
    "v_mfma_f32_32x32x16_bf16 v[32:47], v[176:179], v[192:195], v[144:159]\n\t"
    "ds_read_b128 v[176:179], v204 offset:43008\n\t"
    "v_exp_f32 v133, v133\n\t"
    "v_exp_f32 v134, v134\n\t"
    "v_exp_f32 v135, v135\n\t"
    "s_lshl_b32 s30, s8, 8\n\t"
    "s_add_u32 s30, s30, 0x1004000\n\t"
    "v_and_b32 v202, 31, v1\n\t"
    "v_lshlrev_b32 v202, 3, v202\n\t"
    "v_add_u32 v202, s30, v202\n\t"
    "buffer_load_dwordx2 v[200:201], v202, s[52:55], 0 offen sc1\n\t"
    "v_exp_f32 v136, v136\n\t"
    "v_exp_f32 v137, v137\n\t"
    "s_waitcnt lgkmcnt(2)\n\t"
    "v_smfmac_f32_32x32x32_bf16 v[160:175], v[180:183], v[128:135], v206\n\t"
    "s_mov_b64 exec, s[18:19]\n\t"
    "ds_read_b128 v[180:183], v205 offset:41984\n\t"
    "ds_read_b128 v[188:191], v205 offset:42496\n\t"
    "s_mov_b64 exec, -1\n\t"
    "v_exp_f32 v138, v138\n\t"
    "v_exp_f32 v139, v139\n\t"
    "v_exp_f32 v140, v140\n\t"
    "s_waitcnt vmcnt(7)\n\t"
    "v_mfma_f32_32x32x16_bf16 v[96:111], v[24:27], v[52:55], 0\n\t"
    "v_exp_f32 v141, v141\n\t"
    "v_exp_f32 v142, v142\n\t"
    "v_exp_f32 v143, v143\n\t"
    "v_exp_f32 v32, v32\n\t"
    "v_exp_f32 v33, v33\n\t"
    "s_waitcnt lgkmcnt(3)\n\t"
    "v_smfmac_f32_32x32x32_bf16 v[160:175], v[184:187], v[136:143], v206\n\t"
    "v_exp_f32 v34, v34\n\t"
    "v_exp_f32 v35, v35\n\t"
    "v_exp_f32 v36, v36\n\t"
    "s_waitcnt lgkmcnt(2)\n\t"
    "v_mfma_f32_32x32x16_bf16 v[128:143], v[176:179], v[192:195], v[144:159]\n\t"
    "ds_read_b128 v[176:179], v204 offset:44032\n\t"
    "v_exp_f32 v37, v37\n\t"
    "v_exp_f32 v38, v38\n\t"
    "v_exp_f32 v39, v39\n\t"
    "v_exp_f32 v40, v40\n\t"
    "v_exp_f32 v41, v41\n\t"
    "s_waitcnt lgkmcnt(2)\n\t"
    "v_smfmac_f32_32x32x32_bf16 v[160:175], v[180:183], v[32:39], v206\n\t"
    "s_mov_b64 exec, s[18:19]\n\t"
    "ds_read_b128 v[180:183], v205 offset:43008\n\t"
    "ds_read_b128 v[184:187], v205 offset:43520\n\t"
    "s_mov_b64 exec, -1\n\t"
    "v_exp_f32 v42, v42\n\t"
    "v_exp_f32 v43, v43\n\t"
    "v_exp_f32 v44, v44\n\t"
    "v_mfma_f32_32x32x16_bf16 v[112:127], v[28:31], v[52:55], 0\n\t"
    "v_exp_f32 v45, v45\n\t"
    "v_exp_f32 v46, v46\n\t"
    "v_exp_f32 v47, v47\n\t"
    "v_exp_f32 v128, v128\n\t"
    "v_exp_f32 v129, v129\n\t"
    "s_waitcnt lgkmcnt(3)\n\t"
    "v_smfmac_f32_32x32x32_bf16 v[160:175], v[188:191], v[40:47], v206\n\t"
    "v_exp_f32 v130, v130\n\t"
    "ds_read_b64_tr_b16 v[24:25], v252 offset:2048\n\t"
    "ds_read_b64_tr_b16 v[26:27], v252 offset:2560\n\t"
    "ds_read_b64_tr_b16 v[28:29], v253 offset:2048\n\t"
    "ds_read_b64_tr_b16 v[30:31], v253 offset:2560\n\t"
    "v_exp_f32 v131, v131\n\t"
    "v_exp_f32 v132, v132\n\t"
    "s_waitcnt lgkmcnt(6)\n\t"
    "v_mfma_f32_32x32x16_bf16 v[32:47], v[176:179], v[192:195], v[144:159]\n\t"
    "ds_read_b128 v[176:179], v204 offset:45056\n\t"
    "v_exp_f32 v133, v133\n\t"
    "s_mov_b32 s11, 0x6000\n\t"
    "buffer_load_dwordx4 v[48:51], v20, s[52:55], s11 offen sc1\n\t"
    "v_exp_f32 v134, v134\n\t"
    "v_exp_f32 v135, v135\n\t"
    "v_exp_f32 v136, v136\n\t"
    "v_exp_f32 v137, v137\n\t"
    "s_waitcnt lgkmcnt(6)\n\t"
    "v_smfmac_f32_32x32x32_bf16 v[160:175], v[180:183], v[128:135], v206\n\t"
    "s_mov_b64 exec, s[18:19]\n\t"
    "ds_read_b128 v[180:183], v205 offset:44032\n\t"
    "ds_read_b128 v[188:191], v205 offset:44544\n\t"
    "s_mov_b64 exec, -1\n\t"
    "v_exp_f32 v138, v138\n\t"
    "v_exp_f32 v139, v139\n\t"
    "v_exp_f32 v140, v140\n\t"
    "s_waitcnt vmcnt(7) lgkmcnt(5)\n\t"
    "v_mfma_f32_32x32x16_bf16 v[64:79], v[24:27], v[56:59], v[64:79]\n\t"
    "v_exp_f32 v141, v141\n\t"
    "v_exp_f32 v142, v142\n\t"
    "s_mov_b32 s11, 0x2a000\n\t"
    "buffer_load_dwordx4 v[52:55], v20, s[52:55], s11 offen sc1\n\t"
    "v_exp_f32 v143, v143\n\t"
    "v_exp_f32 v32, v32\n\t"
    "v_exp_f32 v33, v33\n\t"
    "v_smfmac_f32_32x32x32_bf16 v[160:175], v[184:187], v[136:143], v206\n\t"
    "v_exp_f32 v34, v34\n\t"
    "v_exp_f32 v35, v35\n\t"
    "v_exp_f32 v36, v36\n\t"
    "s_waitcnt lgkmcnt(2)\n\t"
    "v_mfma_f32_32x32x16_bf16 v[128:143], v[176:179], v[192:195], v[144:159]\n\t"
    "ds_read_b128 v[176:179], v204 offset:46080\n\t"
    "v_exp_f32 v37, v37\n\t"
    "v_exp_f32 v38, v38\n\t"
    "v_exp_f32 v39, v39\n\t"
    "s_waitcnt vmcnt(2)\n\t"
    "v_cmp_ne_u64 vcc, s[82:83], v[200:201]\n\t"
    "s_nop 1\n\t"
    "s_cbranch_vccnz L_sa_retry_%=\n\t"
    "L_sa_ok_%=:\n\t"
    "v_exp_f32 v40, v40\n\t"
    "v_exp_f32 v41, v41\n\t"
    "s_waitcnt lgkmcnt(2)\n\t"
    "v_smfmac_f32_32x32x32_bf16 v[160:175], v[180:183], v[32:39], v206\n\t"
    "s_mov_b64 exec, s[18:19]\n\t"
    "ds_read_b128 v[180:183], v205 offset:45056\n\t"
    "ds_read_b128 v[184:187], v205 offset:45568\n\t"
    "s_mov_b64 exec, -1\n\t"
    "v_exp_f32 v42, v42\n\t"
    "v_exp_f32 v43, v43\n\t"
    "v_exp_f32 v44, v44\n\t"
    "v_mfma_f32_32x32x16_bf16 v[80:95], v[28:31], v[56:59], v[80:95]\n\t"
    "v_exp_f32 v45, v45\n\t"
    "s_add_u32 m0, s15, 0x800\n\t"
    "s_mov_b32 s4, 0x60000\n\t"
    "buffer_load_dwordx4 v207, s[56:59], s4 offen lds\n\t"
    "v_exp_f32 v46, v46\n\t"
    "v_exp_f32 v47, v47\n\t"
    "v_exp_f32 v128, v128\n\t"
    "v_exp_f32 v129, v129\n\t"
    "s_waitcnt lgkmcnt(3)\n\t"
    "v_smfmac_f32_32x32x32_bf16 v[160:175], v[188:191], v[40:47], v206\n\t"
    "v_exp_f32 v130, v130\n\t"
    "v_exp_f32 v131, v131\n\t"
    "s_add_u32 m0, s15, 0xc00\n\t"
    "s_mov_b32 s4, 0x62000\n\t"
    "buffer_load_dwordx4 v207, s[56:59], s4 offen lds\n\t"
    "v_exp_f32 v132, v132\n\t"
    "s_waitcnt lgkmcnt(2)\n\t"
    "v_mfma_f32_32x32x16_bf16 v[32:47], v[176:179], v[192:195], v[144:159]\n\t"
    "ds_read_b128 v[176:179], v204 offset:47104\n\t"
    "v_exp_f32 v133, v133\n\t"
    "v_exp_f32 v134, v134\n\t"
    "v_exp_f32 v135, v135\n\t"
    "v_exp_f32 v136, v136\n\t"
    "v_exp_f32 v137, v137\n\t"
    "s_waitcnt lgkmcnt(2)\n\t"
    "v_smfmac_f32_32x32x32_bf16 v[160:175], v[180:183], v[128:135], v206\n\t"
    "s_mov_b64 exec, s[18:19]\n\t"
    "ds_read_b128 v[180:183], v205 offset:46080\n\t"
    "ds_read_b128 v[188:191], v205 offset:46592\n\t"
    "s_mov_b64 exec, -1\n\t"
    "v_exp_f32 v138, v138\n\t"
    "v_exp_f32 v139, v139\n\t"
    "v_exp_f32 v140, v140\n\t"
    "v_mfma_f32_32x32x16_bf16 v[96:111], v[24:27], v[60:63], v[96:111]\n\t"
    "v_exp_f32 v141, v141\n\t"
    "v_exp_f32 v142, v142\n\t"
    "v_exp_f32 v143, v143\n\t"
    "v_exp_f32 v32, v32\n\t"
    "v_exp_f32 v33, v33\n\t"
    "s_waitcnt lgkmcnt(3)\n\t"
    "v_smfmac_f32_32x32x32_bf16 v[160:175], v[184:187], v[136:143], v206\n\t"
    "v_exp_f32 v34, v34\n\t"
    "v_exp_f32 v35, v35\n\t"
    "v_exp_f32 v36, v36\n\t"
    "s_waitcnt lgkmcnt(2)\n\t"
    "v_mfma_f32_32x32x16_bf16 v[128:143], v[176:179], v[192:195], v[144:159]\n\t"
    "ds_read_b128 v[176:179], v204 offset:48128\n\t"
    "v_exp_f32 v37, v37\n\t"
    "v_exp_f32 v38, v38\n\t"
    "v_exp_f32 v39, v39\n\t"
    "v_exp_f32 v40, v40\n\t"
    "v_exp_f32 v41, v41\n\t"
    "s_waitcnt lgkmcnt(2)\n\t"
    "v_smfmac_f32_32x32x32_bf16 v[160:175], v[180:183], v[32:39], v206\n\t"
    "s_mov_b64 exec, s[18:19]\n\t"
    "ds_read_b128 v[180:183], v205 offset:47104\n\t"
    "ds_read_b128 v[184:187], v205 offset:47616\n\t"
    "s_mov_b64 exec, -1\n\t"
    "v_exp_f32 v42, v42\n\t"
    "v_exp_f32 v43, v43\n\t"
    "v_exp_f32 v44, v44\n\t"
    "v_mfma_f32_32x32x16_bf16 v[112:127], v[28:31], v[60:63], v[112:127]\n\t"
    "v_exp_f32 v45, v45\n\t"
    "v_exp_f32 v46, v46\n\t"
    "v_exp_f32 v47, v47\n\t"
    "v_exp_f32 v128, v128\n\t"
    "v_exp_f32 v129, v129\n\t"
    "s_waitcnt lgkmcnt(3)\n\t"
    "v_smfmac_f32_32x32x32_bf16 v[160:175], v[188:191], v[40:47], v206\n\t"
    "v_exp_f32 v130, v130\n\t"
    "ds_read_b64_tr_b16 v[24:25], v252\n\t"
    "ds_read_b64_tr_b16 v[26:27], v252 offset:512\n\t"
    "ds_read_b64_tr_b16 v[28:29], v253\n\t"
    "ds_read_b64_tr_b16 v[30:31], v253 offset:512\n\t"
    "v_exp_f32 v131, v131\n\t"
    "v_exp_f32 v132, v132\n\t"
    "s_waitcnt lgkmcnt(6)\n\t"
    "v_mfma_f32_32x32x16_bf16 v[32:47], v[176:179], v[192:195], v[144:159]\n\t"
    "v_add_u32 v204, 0x10000, v204\n\t"
    "ds_read_b128 v[176:179], v204\n\t"
    "v_exp_f32 v133, v133\n\t"
    "s_mov_b32 s11, 0x8000\n\t"
    "buffer_load_dwordx4 v[56:59], v20, s[52:55], s11 offen sc1\n\t"
    "v_exp_f32 v134, v134\n\t"
    "v_exp_f32 v135, v135\n\t"
    "v_exp_f32 v136, v136\n\t"
    "v_exp_f32 v137, v137\n\t"
    "s_waitcnt lgkmcnt(6)\n\t"
    "v_smfmac_f32_32x32x32_bf16 v[160:175], v[180:183], v[128:135], v206\n\t"
    "s_mov_b64 exec, s[18:19]\n\t"
    "ds_read_b128 v[180:183], v205 offset:48128\n\t"
    "ds_read_b128 v[188:191], v205 offset:48640\n\t"
    "s_mov_b64 exec, -1\n\t"
    "v_exp_f32 v138, v138\n\t"
    "v_exp_f32 v139, v139\n\t"
    "v_exp_f32 v140, v140\n\t"
    "s_waitcnt lgkmcnt(5)\n\t"
    "v_mfma_f32_32x32x16_bf16 v[64:79], v[24:27], v[2:5], v[64:79]\n\t"
    "v_exp_f32 v141, v141\n\t"
    "v_exp_f32 v142, v142\n\t"
    "s_mov_b32 s11, 0x2c000\n\t"
    "buffer_load_dwordx4 v[60:63], v20, s[52:55], s11 offen sc1\n\t"
    "v_exp_f32 v143, v143\n\t"
    "v_exp_f32 v32, v32\n\t"
    "v_exp_f32 v33, v33\n\t"
    "v_smfmac_f32_32x32x32_bf16 v[160:175], v[184:187], v[136:143], v206\n\t"
    "v_exp_f32 v34, v34\n\t"
    "v_exp_f32 v35, v35\n\t"
    "v_exp_f32 v36, v36\n\t"
    "s_waitcnt lgkmcnt(2)\n\t"
    "v_mfma_f32_32x32x16_bf16 v[128:143], v[176:179], v[192:195], v[144:159]\n\t"
    "ds_read_b128 v[176:179], v204 offset:1024\n\t"
    "v_exp_f32 v37, v37\n\t"
    "v_exp_f32 v38, v38\n\t"
    "v_exp_f32 v39, v39\n\t"
    "v_exp_f32 v40, v40\n\t"
    "v_exp_f32 v41, v41\n\t"
    "s_waitcnt lgkmcnt(2)\n\t"
    "v_smfmac_f32_32x32x32_bf16 v[160:175], v[180:183], v[32:39], v206\n\t"
    "v_add_u32 v205, 0x10000, v205\n\t"
    "s_mov_b64 exec, s[18:19]\n\t"
    "ds_read_b128 v[180:183], v205\n\t"
    "ds_read_b128 v[184:187], v205 offset:512\n\t"
    "s_mov_b64 exec, -1\n\t"
    "v_exp_f32 v42, v42\n\t"
    "v_exp_f32 v43, v43\n\t"
    "v_exp_f32 v44, v44\n\t"
    "v_mfma_f32_32x32x16_bf16 v[80:95], v[28:31], v[2:5], v[80:95]\n\t"
    "v_exp_f32 v45, v45\n\t"
    "s_add_u32 m0, s15, 0\n\t"
    "s_mov_b32 s4, 0x80000\n\t"
    "buffer_load_dwordx4 v207, s[56:59], s4 offen lds\n\t"
    "v_exp_f32 v46, v46\n\t"
    "v_exp_f32 v47, v47\n\t"
    "v_exp_f32 v128, v128\n\t"
    "v_exp_f32 v129, v129\n\t"
    "s_waitcnt lgkmcnt(3)\n\t"
    "v_smfmac_f32_32x32x32_bf16 v[160:175], v[188:191], v[40:47], v206\n\t"
    "v_exp_f32 v130, v130\n\t"
    "v_exp_f32 v131, v131\n\t"
    "s_add_u32 m0, s15, 0x400\n\t"
    "s_mov_b32 s4, 0x82000\n\t"
    "buffer_load_dwordx4 v207, s[56:59], s4 offen lds\n\t"
    "v_exp_f32 v132, v132\n\t"
    "s_waitcnt lgkmcnt(2)\n\t"
    "v_mfma_f32_32x32x16_bf16 v[32:47], v[176:179], v[192:195], v[144:159]\n\t"
    "ds_read_b128 v[176:179], v204 offset:2048\n\t"
    "v_exp_f32 v133, v133\n\t"
    "v_exp_f32 v134, v134\n\t"
    "v_exp_f32 v135, v135\n\t"
    "v_exp_f32 v136, v136\n\t"
    "v_exp_f32 v137, v137\n\t"
    "s_waitcnt lgkmcnt(2)\n\t"
    "v_smfmac_f32_32x32x32_bf16 v[160:175], v[180:183], v[128:135], v206\n\t"
    "s_mov_b64 exec, s[18:19]\n\t"
    "ds_read_b128 v[180:183], v205 offset:1024\n\t"
    "ds_read_b128 v[188:191], v205 offset:1536\n\t"
    "s_mov_b64 exec, -1\n\t"
    "v_exp_f32 v138, v138\n\t"
    "v_exp_f32 v139, v139\n\t"
    "v_exp_f32 v140, v140\n\t"
    "v_mfma_f32_32x32x16_bf16 v[96:111], v[24:27], v[6:9], v[96:111]\n\t"
    "v_exp_f32 v141, v141\n\t"
    "v_exp_f32 v142, v142\n\t"
    "v_exp_f32 v143, v143\n\t"
    "v_exp_f32 v32, v32\n\t"
    "v_exp_f32 v33, v33\n\t"
    "s_waitcnt lgkmcnt(3)\n\t"
    "v_smfmac_f32_32x32x32_bf16 v[160:175], v[184:187], v[136:143], v206\n\t"
    "v_exp_f32 v34, v34\n\t"
    "v_exp_f32 v35, v35\n\t"
    "v_exp_f32 v36, v36\n\t"
    "s_waitcnt lgkmcnt(2)\n\t"
    "v_mfma_f32_32x32x16_bf16 v[128:143], v[176:179], v[192:195], v[144:159]\n\t"
    "ds_read_b128 v[176:179], v204 offset:3072\n\t"
    "v_exp_f32 v37, v37\n\t"
    "v_exp_f32 v38, v38\n\t"
    "v_exp_f32 v39, v39\n\t"
    "v_exp_f32 v40, v40\n\t"
    "v_exp_f32 v41, v41\n\t"
    "s_waitcnt lgkmcnt(2)\n\t"
    "v_smfmac_f32_32x32x32_bf16 v[160:175], v[180:183], v[32:39], v206\n\t"
    "s_mov_b64 exec, s[18:19]\n\t"
    "ds_read_b128 v[180:183], v205 offset:2048\n\t"
    "ds_read_b128 v[184:187], v205 offset:2560\n\t"
    "s_mov_b64 exec, -1\n\t"
    "v_exp_f32 v42, v42\n\t"
    "v_exp_f32 v43, v43\n\t"
    "v_exp_f32 v44, v44\n\t"
    "v_mfma_f32_32x32x16_bf16 v[112:127], v[28:31], v[6:9], v[112:127]\n\t"
    "v_exp_f32 v45, v45\n\t"
    "v_exp_f32 v46, v46\n\t"
    "v_exp_f32 v47, v47\n\t"
    "v_exp_f32 v128, v128\n\t"
    "v_exp_f32 v129, v129\n\t"
    "s_waitcnt lgkmcnt(3)\n\t"
    "v_smfmac_f32_32x32x32_bf16 v[160:175], v[188:191], v[40:47], v206\n\t"
    "v_exp_f32 v130, v130\n\t"
    "s_waitcnt vmcnt(4)\n\t"
    "ds_read_b64_tr_b16 v[24:25], v252 offset:2048\n\t"
    "ds_read_b64_tr_b16 v[26:27], v252 offset:2560\n\t"
    "ds_read_b64_tr_b16 v[28:29], v253 offset:2048\n\t"
    "ds_read_b64_tr_b16 v[30:31], v253 offset:2560\n\t"
    "v_exp_f32 v131, v131\n\t"
    "v_exp_f32 v132, v132\n\t"
    "s_waitcnt lgkmcnt(6)\n\t"
    "v_mfma_f32_32x32x16_bf16 v[32:47], v[176:179], v[192:195], v[144:159]\n\t"
    "ds_read_b128 v[176:179], v204 offset:4096\n\t"
    "v_exp_f32 v133, v133\n\t"
    "s_mov_b32 s11, 0xa000\n\t"
    "buffer_load_dwordx4 v[2:5], v20, s[52:55], s11 offen sc1\n\t"
    "v_exp_f32 v134, v134\n\t"
    "v_exp_f32 v135, v135\n\t"
    "v_exp_f32 v136, v136\n\t"
    "v_exp_f32 v137, v137\n\t"
    "s_waitcnt lgkmcnt(6)\n\t"
    "v_smfmac_f32_32x32x32_bf16 v[160:175], v[180:183], v[128:135], v206\n\t"
    "s_mov_b64 exec, s[18:19]\n\t"
    "ds_read_b128 v[180:183], v205 offset:3072\n\t"
    "ds_read_b128 v[188:191], v205 offset:3584\n\t"
    "s_mov_b64 exec, -1\n\t"
    "v_exp_f32 v138, v138\n\t"
    "v_exp_f32 v139, v139\n\t"
    "v_exp_f32 v140, v140\n\t"
    "s_waitcnt lgkmcnt(5)\n\t"
    "v_mfma_f32_32x32x16_bf16 v[64:79], v[24:27], v[48:51], v[64:79]\n\t"
    "v_exp_f32 v141, v141\n\t"
    "v_exp_f32 v142, v142\n\t"
    "s_mov_b32 s11, 0x2e000\n\t"
    "buffer_load_dwordx4 v[6:9], v20, s[52:55], s11 offen sc1\n\t"
    "v_exp_f32 v143, v143\n\t"
    "v_exp_f32 v32, v32\n\t"
    "v_exp_f32 v33, v33\n\t"
    "v_smfmac_f32_32x32x32_bf16 v[160:175], v[184:187], v[136:143], v206\n\t"
    "v_exp_f32 v34, v34\n\t"
    "v_exp_f32 v35, v35\n\t"
    "v_exp_f32 v36, v36\n\t"
    "s_waitcnt lgkmcnt(2)\n\t"
    "v_mfma_f32_32x32x16_bf16 v[128:143], v[176:179], v[192:195], v[144:159]\n\t"
    "ds_read_b128 v[176:179], v204 offset:5120\n\t"
    "v_exp_f32 v37, v37\n\t"
    "v_exp_f32 v38, v38\n\t"
    "s_cmp_ge_u32 s8, 4\n\t"
    "s_cbranch_scc1 L_pr3_%=\n\t"
    "s_setprio 0\n\t"
    "L_pr3_%=:\n\t"
    "v_exp_f32 v39, v39\n\t"
    "v_exp_f32 v40, v40\n\t"
    "v_exp_f32 v41, v41\n\t"
    "s_waitcnt lgkmcnt(2)\n\t"
    "v_smfmac_f32_32x32x32_bf16 v[160:175], v[180:183], v[32:39], v206\n\t"
    "s_mov_b64 exec, s[18:19]\n\t"
    "ds_read_b128 v[180:183], v205 offset:4096\n\t"
    "ds_read_b128 v[184:187], v205 offset:4608\n\t"
    "s_mov_b64 exec, -1\n\t"
    "v_exp_f32 v42, v42\n\t"
    "v_exp_f32 v43, v43\n\t"
    "v_exp_f32 v44, v44\n\t"
    "v_mfma_f32_32x32x16_bf16 v[80:95], v[28:31], v[48:51], v[80:95]\n\t"
    "v_exp_f32 v45, v45\n\t"
    "s_add_u32 m0, s15, 0x800\n\t"
    "s_mov_b32 s4, 0xa0000\n\t"
    "buffer_load_dwordx4 v207, s[56:59], s4 offen lds\n\t"
    "v_exp_f32 v46, v46\n\t"
    "v_exp_f32 v47, v47\n\t"
    "v_exp_f32 v128, v128\n\t"
    "v_exp_f32 v129, v129\n\t"
    "s_waitcnt lgkmcnt(3)\n\t"
    "v_smfmac_f32_32x32x32_bf16 v[160:175], v[188:191], v[40:47], v206\n\t"
    "v_exp_f32 v130, v130\n\t"
    "v_exp_f32 v131, v131\n\t"
    "s_add_u32 m0, s15, 0xc00\n\t"
    "s_mov_b32 s4, 0xa2000\n\t"
    "buffer_load_dwordx4 v207, s[56:59], s4 offen lds\n\t"
    "v_exp_f32 v132, v132\n\t"
    "s_waitcnt lgkmcnt(2)\n\t"
    "v_mfma_f32_32x32x16_bf16 v[32:47], v[176:179], v[192:195], v[144:159]\n\t"
    "ds_read_b128 v[176:179], v204 offset:6144\n\t"
    "v_exp_f32 v133, v133\n\t"
    "v_exp_f32 v134, v134\n\t"
    "v_exp_f32 v135, v135\n\t"
    "v_exp_f32 v136, v136\n\t"
    "v_exp_f32 v137, v137\n\t"
    "s_waitcnt lgkmcnt(2)\n\t"
    "v_smfmac_f32_32x32x32_bf16 v[160:175], v[180:183], v[128:135], v206\n\t"
    "s_mov_b64 exec, s[18:19]\n\t"
    "ds_read_b128 v[180:183], v205 offset:5120\n\t"
    "ds_read_b128 v[188:191], v205 offset:5632\n\t"
    "s_mov_b64 exec, -1\n\t"
    "v_exp_f32 v138, v138\n\t"
    "v_exp_f32 v139, v139\n\t"
    "v_exp_f32 v140, v140\n\t"
    "v_mfma_f32_32x32x16_bf16 v[96:111], v[24:27], v[52:55], v[96:111]\n\t"
    "v_exp_f32 v141, v141\n\t"
    "v_exp_f32 v142, v142\n\t"
    "v_exp_f32 v143, v143\n\t"
    "v_exp_f32 v32, v32\n\t"
    "v_exp_f32 v33, v33\n\t"
    "s_waitcnt lgkmcnt(3)\n\t"
    "v_smfmac_f32_32x32x32_bf16 v[160:175], v[184:187], v[136:143], v206\n\t"
    "v_exp_f32 v34, v34\n\t"
    "v_exp_f32 v35, v35\n\t"
    "v_exp_f32 v36, v36\n\t"
    "s_waitcnt lgkmcnt(2)\n\t"
    "v_mfma_f32_32x32x16_bf16 v[128:143], v[176:179], v[192:195], v[144:159]\n\t"
    "ds_read_b128 v[176:179], v204 offset:7168\n\t"
    "v_exp_f32 v37, v37\n\t"
    "v_exp_f32 v38, v38\n\t"
    "v_exp_f32 v39, v39\n\t"
    "v_exp_f32 v40, v40\n\t"
    "v_exp_f32 v41, v41\n\t"
    "s_waitcnt lgkmcnt(2)\n\t"
    "v_smfmac_f32_32x32x32_bf16 v[160:175], v[180:183], v[32:39], v206\n\t"
    "s_mov_b64 exec, s[18:19]\n\t"
    "ds_read_b128 v[180:183], v205 offset:6144\n\t"
    "ds_read_b128 v[184:187], v205 offset:6656\n\t"
    "s_mov_b64 exec, -1\n\t"
    "v_exp_f32 v42, v42\n\t"
    "v_exp_f32 v43, v43\n\t"
    "v_exp_f32 v44, v44\n\t"
    "v_mfma_f32_32x32x16_bf16 v[112:127], v[28:31], v[52:55], v[112:127]\n\t"
    "v_exp_f32 v45, v45\n\t"
    "v_exp_f32 v46, v46\n\t"
    "v_exp_f32 v47, v47\n\t"
    "v_exp_f32 v128, v128\n\t"
    "v_exp_f32 v129, v129\n\t"
    "s_waitcnt lgkmcnt(3)\n\t"
    "v_smfmac_f32_32x32x32_bf16 v[160:175], v[188:191], v[40:47], v206\n\t"
    "v_exp_f32 v130, v130\n\t"
    "s_waitcnt vmcnt(4)\n\t"
    "ds_read_b64_tr_b16 v[24:25], v252\n\t"
    "ds_read_b64_tr_b16 v[26:27], v252 offset:512\n\t"
    "ds_read_b64_tr_b16 v[28:29], v253\n\t"
    "ds_read_b64_tr_b16 v[30:31], v253 offset:512\n\t"
    "v_exp_f32 v131, v131\n\t"
    "v_exp_f32 v132, v132\n\t"
    "s_waitcnt lgkmcnt(6)\n\t"
    "v_mfma_f32_32x32x16_bf16 v[32:47], v[176:179], v[192:195], v[144:159]\n\t"
    "ds_read_b128 v[176:179], v204 offset:8192\n\t"
    "v_exp_f32 v133, v133\n\t"
    "s_mov_b32 s11, 0xc000\n\t"
    "buffer_load_dwordx4 v[48:51], v20, s[52:55], s11 offen sc1\n\t"
    "v_exp_f32 v134, v134\n\t"
    "v_exp_f32 v135, v135\n\t"
    "v_exp_f32 v136, v136\n\t"
    "v_exp_f32 v137, v137\n\t"
    "s_waitcnt lgkmcnt(6)\n\t"
    "v_smfmac_f32_32x32x32_bf16 v[160:175], v[180:183], v[128:135], v206\n\t"
    "s_mov_b64 exec, s[18:19]\n\t"
    "ds_read_b128 v[180:183], v205 offset:7168\n\t"
    "ds_read_b128 v[188:191], v205 offset:7680\n\t"
    "s_mov_b64 exec, -1\n\t"
    "v_exp_f32 v138, v138\n\t"
    "v_exp_f32 v139, v139\n\t"
    "v_exp_f32 v140, v140\n\t"
    "s_waitcnt lgkmcnt(5)\n\t"
    "v_mfma_f32_32x32x16_bf16 v[64:79], v[24:27], v[56:59], v[64:79]\n\t"
    "v_exp_f32 v141, v141\n\t"
    "v_exp_f32 v142, v142\n\t"
    "s_mov_b32 s11, 0x30000\n\t"
    "buffer_load_dwordx4 v[52:55], v20, s[52:55], s11 offen sc1\n\t"
    "v_exp_f32 v143, v143\n\t"
    "v_exp_f32 v32, v32\n\t"
    "v_exp_f32 v33, v33\n\t"
    "v_smfmac_f32_32x32x32_bf16 v[160:175], v[184:187], v[136:143], v206\n\t"
    "v_exp_f32 v34, v34\n\t"
    "v_exp_f32 v35, v35\n\t"
    "v_exp_f32 v36, v36\n\t"
    "s_waitcnt lgkmcnt(2)\n\t"
    "v_mfma_f32_32x32x16_bf16 v[128:143], v[176:179], v[192:195], v[144:159]\n\t"
    "ds_read_b128 v[176:179], v204 offset:9216\n\t"
    "v_exp_f32 v37, v37\n\t"
    "v_exp_f32 v38, v38\n\t"
    "v_exp_f32 v39, v39\n\t"
    "v_exp_f32 v40, v40\n\t"
    "v_exp_f32 v41, v41\n\t"
    "s_waitcnt lgkmcnt(2)\n\t"
    "v_smfmac_f32_32x32x32_bf16 v[160:175], v[180:183], v[32:39], v206\n\t"
    "s_mov_b64 exec, s[18:19]\n\t"
    "ds_read_b128 v[180:183], v205 offset:8192\n\t"
    "ds_read_b128 v[184:187], v205 offset:8704\n\t"
    "s_mov_b64 exec, -1\n\t"
    "v_exp_f32 v42, v42\n\t"
    "v_exp_f32 v43, v43\n\t"
    "v_exp_f32 v44, v44\n\t"
    "v_mfma_f32_32x32x16_bf16 v[80:95], v[28:31], v[56:59], v[80:95]\n\t"
    "v_exp_f32 v45, v45\n\t"
    "s_add_u32 m0, s15, 0\n\t"
    "s_mov_b32 s4, 0xc0000\n\t"
    "buffer_load_dwordx4 v207, s[56:59], s4 offen lds\n\t"
    "v_exp_f32 v46, v46\n\t"
    "v_exp_f32 v47, v47\n\t"
    "v_exp_f32 v128, v128\n\t"
    "v_exp_f32 v129, v129\n\t"
    "s_waitcnt lgkmcnt(3)\n\t"
    "v_smfmac_f32_32x32x32_bf16 v[160:175], v[188:191], v[40:47], v206\n\t"
    "v_exp_f32 v130, v130\n\t"
    "v_exp_f32 v131, v131\n\t"
    "s_add_u32 m0, s15, 0x400\n\t"
    "s_mov_b32 s4, 0xc2000\n\t"
    "buffer_load_dwordx4 v207, s[56:59], s4 offen lds\n\t"
    "v_exp_f32 v132, v132\n\t"
    "s_waitcnt lgkmcnt(2)\n\t"
    "v_mfma_f32_32x32x16_bf16 v[32:47], v[176:179], v[192:195], v[144:159]\n\t"
    "ds_read_b128 v[176:179], v204 offset:10240\n\t"
    "v_exp_f32 v133, v133\n\t"
    "v_exp_f32 v134, v134\n\t"
    "v_exp_f32 v135, v135\n\t"
    "v_exp_f32 v136, v136\n\t"
    "v_exp_f32 v137, v137\n\t"
    "s_waitcnt lgkmcnt(2)\n\t"
    "v_smfmac_f32_32x32x32_bf16 v[160:175], v[180:183], v[128:135], v206\n\t"
    "s_mov_b64 exec, s[18:19]\n\t"
    "ds_read_b128 v[180:183], v205 offset:9216\n\t"
    "ds_read_b128 v[188:191], v205 offset:9728\n\t"
    "s_mov_b64 exec, -1\n\t"
    "v_exp_f32 v138, v138\n\t"
    "v_exp_f32 v139, v139\n\t"
    "v_exp_f32 v140, v140\n\t"
    "v_mfma_f32_32x32x16_bf16 v[96:111], v[24:27], v[60:63], v[96:111]\n\t"
    "v_exp_f32 v141, v141\n\t"
    "v_exp_f32 v142, v142\n\t"
    "v_exp_f32 v143, v143\n\t"
    "v_exp_f32 v32, v32\n\t"
    "v_exp_f32 v33, v33\n\t"
    "s_waitcnt lgkmcnt(3)\n\t"
    "v_smfmac_f32_32x32x32_bf16 v[160:175], v[184:187], v[136:143], v206\n\t"
    "v_exp_f32 v34, v34\n\t"
    "v_exp_f32 v35, v35\n\t"
    "v_exp_f32 v36, v36\n\t"
    "s_waitcnt lgkmcnt(2)\n\t"
    "v_mfma_f32_32x32x16_bf16 v[128:143], v[176:179], v[192:195], v[144:159]\n\t"
    "ds_read_b128 v[176:179], v204 offset:11264\n\t"
    "v_exp_f32 v37, v37\n\t"
    "v_exp_f32 v38, v38\n\t"
    "v_exp_f32 v39, v39\n\t"
    "v_exp_f32 v40, v40\n\t"
    "v_exp_f32 v41, v41\n\t"
    "s_waitcnt lgkmcnt(2)\n\t"
    "v_smfmac_f32_32x32x32_bf16 v[160:175], v[180:183], v[32:39], v206\n\t"
    "s_mov_b64 exec, s[18:19]\n\t"
    "ds_read_b128 v[180:183], v205 offset:10240\n\t"
    "ds_read_b128 v[184:187], v205 offset:10752\n\t"
    "s_mov_b64 exec, -1\n\t"
    "v_exp_f32 v42, v42\n\t"
    "v_exp_f32 v43, v43\n\t"
    "v_exp_f32 v44, v44\n\t"
    "v_mfma_f32_32x32x16_bf16 v[112:127], v[28:31], v[60:63], v[112:127]\n\t"
    "v_exp_f32 v45, v45\n\t"
    "v_exp_f32 v46, v46\n\t"
    "v_exp_f32 v47, v47\n\t"
    "v_exp_f32 v128, v128\n\t"
    "v_exp_f32 v129, v129\n\t"
    "s_waitcnt lgkmcnt(3)\n\t"
    "v_smfmac_f32_32x32x32_bf16 v[160:175], v[188:191], v[40:47], v206\n\t"
    "v_exp_f32 v130, v130\n\t"
    "s_waitcnt vmcnt(4)\n\t"
    "ds_read_b64_tr_b16 v[24:25], v252 offset:2048\n\t"
    "ds_read_b64_tr_b16 v[26:27], v252 offset:2560\n\t"
    "ds_read_b64_tr_b16 v[28:29], v253 offset:2048\n\t"
    "ds_read_b64_tr_b16 v[30:31], v253 offset:2560\n\t"
    "v_exp_f32 v131, v131\n\t"
    "v_exp_f32 v132, v132\n\t"
    "s_waitcnt lgkmcnt(6)\n\t"
    "v_mfma_f32_32x32x16_bf16 v[32:47], v[176:179], v[192:195], v[144:159]\n\t"
    "ds_read_b128 v[176:179], v204 offset:12288\n\t"
    "v_exp_f32 v133, v133\n\t"
    "s_mov_b32 s11, 0xe000\n\t"
    "buffer_load_dwordx4 v[56:59], v20, s[52:55], s11 offen sc1\n\t"
    "v_exp_f32 v134, v134\n\t"
    "v_exp_f32 v135, v135\n\t"
    "v_exp_f32 v136, v136\n\t"
    "v_exp_f32 v137, v137\n\t"
    "s_waitcnt lgkmcnt(6)\n\t"
    "v_smfmac_f32_32x32x32_bf16 v[160:175], v[180:183], v[128:135], v206\n\t"
    "s_mov_b64 exec, s[18:19]\n\t"
    "ds_read_b128 v[180:183], v205 offset:11264\n\t"
    "ds_read_b128 v[188:191], v205 offset:11776\n\t"
    "s_mov_b64 exec, -1\n\t"
    "v_exp_f32 v138, v138\n\t"
    "v_exp_f32 v139, v139\n\t"
    "v_exp_f32 v140, v140\n\t"
    "s_waitcnt lgkmcnt(5)\n\t"
    "v_mfma_f32_32x32x16_bf16 v[64:79], v[24:27], v[2:5], v[64:79]\n\t"
    "v_exp_f32 v141, v141\n\t"
    "v_exp_f32 v142, v142\n\t"
    "s_mov_b32 s11, 0x32000\n\t"
    "buffer_load_dwordx4 v[60:63], v20, s[52:55], s11 offen sc1\n\t"
    "v_exp_f32 v143, v143\n\t"
    "v_exp_f32 v32, v32\n\t"
    "v_exp_f32 v33, v33\n\t"
    "v_smfmac_f32_32x32x32_bf16 v[160:175], v[184:187], v[136:143], v206\n\t"
    "v_exp_f32 v34, v34\n\t"
    "v_exp_f32 v35, v35\n\t"
    "v_exp_f32 v36, v36\n\t"
    "s_waitcnt lgkmcnt(2)\n\t"
    "v_mfma_f32_32x32x16_bf16 v[128:143], v[176:179], v[192:195], v[144:159]\n\t"
    "ds_read_b128 v[176:179], v204 offset:13312\n\t"
    "v_exp_f32 v37, v37\n\t"
    "v_exp_f32 v38, v38\n\t"
    "v_exp_f32 v39, v39\n\t"
    "v_exp_f32 v40, v40\n\t"
    "v_exp_f32 v41, v41\n\t"
    "s_waitcnt lgkmcnt(2)\n\t"
    "v_smfmac_f32_32x32x32_bf16 v[160:175], v[180:183], v[32:39], v206\n\t"
    "s_mov_b64 exec, s[18:19]\n\t"
    "ds_read_b128 v[180:183], v205 offset:12288\n\t"
    "ds_read_b128 v[184:187], v205 offset:12800\n\t"
    "s_mov_b64 exec, -1\n\t"
    "v_exp_f32 v42, v42\n\t"
    "v_exp_f32 v43, v43\n\t"
    "v_exp_f32 v44, v44\n\t"
    "v_mfma_f32_32x32x16_bf16 v[80:95], v[28:31], v[2:5], v[80:95]\n\t"
    "v_exp_f32 v45, v45\n\t"
    "s_add_u32 m0, s15, 0x800\n\t"
    "s_mov_b32 s4, 0xe0000\n\t"
    "buffer_load_dwordx4 v207, s[56:59], s4 offen lds\n\t"
    "v_exp_f32 v46, v46\n\t"
    "v_exp_f32 v47, v47\n\t"
    "v_exp_f32 v128, v128\n\t"
    "v_exp_f32 v129, v129\n\t"
    "s_waitcnt lgkmcnt(3)\n\t"
    "v_smfmac_f32_32x32x32_bf16 v[160:175], v[188:191], v[40:47], v206\n\t"
    "v_exp_f32 v130, v130\n\t"
    "v_exp_f32 v131, v131\n\t"
    "s_add_u32 m0, s15, 0xc00\n\t"
    "s_mov_b32 s4, 0xe2000\n\t"
    "buffer_load_dwordx4 v207, s[56:59], s4 offen lds\n\t"
    "v_exp_f32 v132, v132\n\t"
    "s_waitcnt lgkmcnt(2)\n\t"
    "v_mfma_f32_32x32x16_bf16 v[32:47], v[176:179], v[192:195], v[144:159]\n\t"
    "ds_read_b128 v[176:179], v204 offset:14336\n\t"
    "v_exp_f32 v133, v133\n\t"
    "v_exp_f32 v134, v134\n\t"
    "v_exp_f32 v135, v135\n\t"
    "v_exp_f32 v136, v136\n\t"
    "v_exp_f32 v137, v137\n\t"
    "s_waitcnt lgkmcnt(2)\n\t"
    "v_smfmac_f32_32x32x32_bf16 v[160:175], v[180:183], v[128:135], v206\n\t"
    "s_mov_b64 exec, s[18:19]\n\t"
    "ds_read_b128 v[180:183], v205 offset:13312\n\t"
    "ds_read_b128 v[188:191], v205 offset:13824\n\t"
    "s_mov_b64 exec, -1\n\t"
    "v_exp_f32 v138, v138\n\t"
    "v_exp_f32 v139, v139\n\t"
    "v_exp_f32 v140, v140\n\t"
    "v_mfma_f32_32x32x16_bf16 v[96:111], v[24:27], v[6:9], v[96:111]\n\t"
    "v_exp_f32 v141, v141\n\t"
    "v_exp_f32 v142, v142\n\t"
    "v_exp_f32 v143, v143\n\t"
    "v_exp_f32 v32, v32\n\t"
    "v_exp_f32 v33, v33\n\t"
    "s_waitcnt lgkmcnt(3)\n\t"
    "v_smfmac_f32_32x32x32_bf16 v[160:175], v[184:187], v[136:143], v206\n\t"
    "v_exp_f32 v34, v34\n\t"
    "v_exp_f32 v35, v35\n\t"
    "v_exp_f32 v36, v36\n\t"
    "s_waitcnt lgkmcnt(2)\n\t"
    "v_mfma_f32_32x32x16_bf16 v[128:143], v[176:179], v[192:195], v[144:159]\n\t"
    "ds_read_b128 v[176:179], v204 offset:15360\n\t"
    "v_exp_f32 v37, v37\n\t"
    "v_exp_f32 v38, v38\n\t"
    "v_exp_f32 v39, v39\n\t"
    "v_exp_f32 v40, v40\n\t"
    "v_exp_f32 v41, v41\n\t"
    "s_waitcnt lgkmcnt(2)\n\t"
    "v_smfmac_f32_32x32x32_bf16 v[160:175], v[180:183], v[32:39], v206\n\t"
    "s_mov_b64 exec, s[18:19]\n\t"
    "ds_read_b128 v[180:183], v205 offset:14336\n\t"
    "ds_read_b128 v[184:187], v205 offset:14848\n\t"
    "s_mov_b64 exec, -1\n\t"
    "v_exp_f32 v42, v42\n\t"
    "v_exp_f32 v43, v43\n\t"
    "v_exp_f32 v44, v44\n\t"
    "v_mfma_f32_32x32x16_bf16 v[112:127], v[28:31], v[6:9], v[112:127]\n\t"
    "v_exp_f32 v45, v45\n\t"
    "v_exp_f32 v46, v46\n\t"
    "v_exp_f32 v47, v47\n\t"
    "v_exp_f32 v128, v128\n\t"
    "v_exp_f32 v129, v129\n\t"
    "s_waitcnt lgkmcnt(3)\n\t"
    "v_smfmac_f32_32x32x32_bf16 v[160:175], v[188:191], v[40:47], v206\n\t"
    "v_exp_f32 v130, v130\n\t"
    "s_waitcnt vmcnt(4)\n\t"
    "ds_read_b64_tr_b16 v[24:25], v252\n\t"
    "ds_read_b64_tr_b16 v[26:27], v252 offset:512\n\t"
    "ds_read_b64_tr_b16 v[28:29], v253\n\t"
    "ds_read_b64_tr_b16 v[30:31], v253 offset:512\n\t"
    "v_exp_f32 v131, v131\n\t"
    "v_exp_f32 v132, v132\n\t"
    "s_waitcnt lgkmcnt(6)\n\t"
    "v_mfma_f32_32x32x16_bf16 v[32:47], v[176:179], v[192:195], v[144:159]\n\t"
    "ds_read_b128 v[176:179], v204 offset:32768\n\t"
    "v_exp_f32 v133, v133\n\t"
    "s_mov_b32 s11, 0x10000\n\t"
    "buffer_load_dwordx4 v[2:5], v20, s[52:55], s11 offen sc1\n\t"
    "v_exp_f32 v134, v134\n\t"
    "v_exp_f32 v135, v135\n\t"
    "v_exp_f32 v136, v136\n\t"
    "v_exp_f32 v137, v137\n\t"
    "s_waitcnt lgkmcnt(6)\n\t"
    "v_smfmac_f32_32x32x32_bf16 v[160:175], v[180:183], v[128:135], v206\n\t"
    "s_mov_b64 exec, s[18:19]\n\t"
    "ds_read_b128 v[180:183], v205 offset:15360\n\t"
    "ds_read_b128 v[188:191], v205 offset:15872\n\t"
    "s_mov_b64 exec, -1\n\t"
    "v_exp_f32 v138, v138\n\t"
    "v_exp_f32 v139, v139\n\t"
    "v_exp_f32 v140, v140\n\t"
    "s_waitcnt lgkmcnt(5)\n\t"
    "v_mfma_f32_32x32x16_bf16 v[64:79], v[24:27], v[48:51], v[64:79]\n\t"
    "v_exp_f32 v141, v141\n\t"
    "v_exp_f32 v142, v142\n\t"
    "s_mov_b32 s11, 0x34000\n\t"
    "buffer_load_dwordx4 v[6:9], v20, s[52:55], s11 offen sc1\n\t"
    "v_exp_f32 v143, v143\n\t"
    "v_exp_f32 v32, v32\n\t"
    "v_exp_f32 v33, v33\n\t"
    "v_smfmac_f32_32x32x32_bf16 v[160:175], v[184:187], v[136:143], v206\n\t"
    "v_exp_f32 v34, v34\n\t"
    "v_exp_f32 v35, v35\n\t"
    "v_exp_f32 v36, v36\n\t"
    "s_waitcnt lgkmcnt(2)\n\t"
    "v_mfma_f32_32x32x16_bf16 v[128:143], v[176:179], v[192:195], v[144:159]\n\t"
    "ds_read_b128 v[176:179], v204 offset:33792\n\t"
    "v_exp_f32 v37, v37\n\t"
    "v_exp_f32 v38, v38\n\t"
    "v_exp_f32 v39, v39\n\t"
    "v_exp_f32 v40, v40\n\t"
    "v_exp_f32 v41, v41\n\t"
    "s_waitcnt lgkmcnt(2)\n\t"
    "v_smfmac_f32_32x32x32_bf16 v[160:175], v[180:183], v[32:39], v206\n\t"
    "s_mov_b64 exec, s[18:19]\n\t"
    "ds_read_b128 v[180:183], v205 offset:32768\n\t"
    "ds_read_b128 v[184:187], v205 offset:33280\n\t"
    "s_mov_b64 exec, -1\n\t"
    "v_exp_f32 v42, v42\n\t"
    "v_exp_f32 v43, v43\n\t"
    "v_exp_f32 v44, v44\n\t"
    "v_mfma_f32_32x32x16_bf16 v[80:95], v[28:31], v[48:51], v[80:95]\n\t"
    "v_exp_f32 v45, v45\n\t"
    "s_add_u32 m0, s15, 0\n\t"
    "s_mov_b32 s4, 0x100000\n\t"
    "buffer_load_dwordx4 v207, s[56:59], s4 offen lds\n\t"
    "v_exp_f32 v46, v46\n\t"
    "v_exp_f32 v47, v47\n\t"
    "v_exp_f32 v128, v128\n\t"
    "v_exp_f32 v129, v129\n\t"
    "s_waitcnt lgkmcnt(3)\n\t"
    "v_smfmac_f32_32x32x32_bf16 v[160:175], v[188:191], v[40:47], v206\n\t"
    "v_exp_f32 v130, v130\n\t"
    "s_barrier\n\t"
    "s_cmp_ge_u32 s8, 4\n\t"
    "s_cbranch_scc1 L_pr4_%=\n\t"
    "s_setprio 2\n\t"
    "L_pr4_%=:\n\t"
    "v_exp_f32 v131, v131\n\t"
    "s_add_u32 m0, s15, 0x400\n\t"
    "s_mov_b32 s4, 0x102000\n\t"
    "buffer_load_dwordx4 v207, s[56:59], s4 offen lds\n\t"
    "v_exp_f32 v132, v132\n\t"
    "s_waitcnt lgkmcnt(2)\n\t"
    "v_mfma_f32_32x32x16_bf16 v[32:47], v[176:179], v[192:195], v[144:159]\n\t"
    "ds_read_b128 v[176:179], v204 offset:34816\n\t"
    "v_exp_f32 v133, v133\n\t"
    "v_exp_f32 v134, v134\n\t"
    "v_exp_f32 v135, v135\n\t"
    "s_lshl_b32 s30, s13, 3\n\t"
    "s_add_u32 s30, s30, s8\n\t"
    "v_and_b32 v215, 31, v1\n\t"
    "v_lshlrev_b32 v215, 2, v215\n\t"
    "s_lshl_b32 s30, s30, 7\n\t"
    "s_add_u32 s30, s30, 0x1000000\n\t"
    "buffer_load_dword v196, v215, s[52:55], s30 offen sc1\n\t"
    "s_lshl_b32 s30, s14, 1\n\t"
    "s_lshr_b32 s31, s8, 2\n\t"
    "s_add_u32 s30, s30, s31\n\t"
    "s_lshl_b32 s30, s30, 7\n\t"
    "s_add_u32 s30, s30, 0x1002000\n\t"
    "buffer_load_dword v197, v215, s[52:55], s30 offen sc1\n\t"
    "s_or_b32 s30, %1, s8\n\t"
    "s_cmp_eq_u32 s30, 0\n\t"
    "s_cselect_b64 s[24:25], 1, 0\n\t"
    "s_mov_b32 s30, 0x1005000\n\t"
    "v_mov_b32 v212, s82\n\t"
    "v_mov_b32 v213, s83\n\t"
    "v_mov_b32 v214, s30\n\t"
    "s_mov_b64 exec, s[24:25]\n\t"
    "buffer_store_dwordx2 v[212:213], v214, s[52:55], 0 offen sc1\n\t"
    "s_mov_b64 exec, -1\n\t"
    "v_exp_f32 v136, v136\n\t"
    "v_exp_f32 v137, v137\n\t"
    "s_waitcnt lgkmcnt(2)\n\t"
    "v_smfmac_f32_32x32x32_bf16 v[160:175], v[180:183], v[128:135], v206\n\t"
    "s_mov_b64 exec, s[18:19]\n\t"
    "ds_read_b128 v[180:183], v205 offset:33792\n\t"
    "ds_read_b128 v[188:191], v205 offset:34304\n\t"
    "s_mov_b64 exec, -1\n\t"
    "v_exp_f32 v138, v138\n\t"
    "v_exp_f32 v139, v139\n\t"
    "v_exp_f32 v140, v140\n\t"
    "v_mfma_f32_32x32x16_bf16 v[96:111], v[24:27], v[52:55], v[96:111]\n\t"
    "v_exp_f32 v141, v141\n\t"
    "v_exp_f32 v142, v142\n\t"
    "v_exp_f32 v143, v143\n\t"
    "v_exp_f32 v32, v32\n\t"
    "v_exp_f32 v33, v33\n\t"
    "s_waitcnt lgkmcnt(3)\n\t"
    "v_smfmac_f32_32x32x32_bf16 v[160:175], v[184:187], v[136:143], v206\n\t"
    "v_exp_f32 v34, v34\n\t"
    "v_exp_f32 v35, v35\n\t"
    "v_exp_f32 v36, v36\n\t"
    "s_waitcnt lgkmcnt(2)\n\t"
    "v_mfma_f32_32x32x16_bf16 v[128:143], v[176:179], v[192:195], v[144:159]\n\t"
    "ds_read_b128 v[176:179], v204 offset:35840\n\t"
    "v_exp_f32 v37, v37\n\t"
    "v_exp_f32 v38, v38\n\t"
    "v_exp_f32 v39, v39\n\t"
    "v_exp_f32 v40, v40\n\t"
    "v_exp_f32 v41, v41\n\t"
    "s_waitcnt lgkmcnt(2)\n\t"
    "v_smfmac_f32_32x32x32_bf16 v[160:175], v[180:183], v[32:39], v206\n\t"
    "s_mov_b64 exec, s[18:19]\n\t"
    "ds_read_b128 v[180:183], v205 offset:34816\n\t"
    "ds_read_b128 v[184:187], v205 offset:35328\n\t"
    "s_mov_b64 exec, -1\n\t"
    "v_exp_f32 v42, v42\n\t"
    "v_exp_f32 v43, v43\n\t"
    "v_exp_f32 v44, v44\n\t"
    "v_mfma_f32_32x32x16_bf16 v[112:127], v[28:31], v[52:55], v[112:127]\n\t"
    "v_exp_f32 v45, v45\n\t"
    "v_exp_f32 v46, v46\n\t"
    "v_exp_f32 v47, v47\n\t"
    "s_add_u32 m0, s27, 0\n\t"
    "s_mov_b32 s4, 0\n\t"
    "buffer_load_dwordx4 v207, s[60:63], s4 offen lds\n\t"
    "v_exp_f32 v128, v128\n\t"
    "v_exp_f32 v129, v129\n\t"
    "s_waitcnt lgkmcnt(3)\n\t"
    "v_smfmac_f32_32x32x32_bf16 v[160:175], v[188:191], v[40:47], v206\n\t"
    "v_exp_f32 v130, v130\n\t"
    "s_waitcnt vmcnt(7)\n\t"
    "ds_read_b64_tr_b16 v[24:25], v252 offset:2048\n\t"
    "ds_read_b64_tr_b16 v[26:27], v252 offset:2560\n\t"
    "ds_read_b64_tr_b16 v[28:29], v253 offset:2048\n\t"
    "ds_read_b64_tr_b16 v[30:31], v253 offset:2560\n\t"
    "v_exp_f32 v131, v131\n\t"
    "v_exp_f32 v132, v132\n\t"
    "s_waitcnt lgkmcnt(6)\n\t"
    "v_mfma_f32_32x32x16_bf16 v[32:47], v[176:179], v[192:195], v[144:159]\n\t"
    "ds_read_b128 v[176:179], v204 offset:36864\n\t"
    "v_exp_f32 v133, v133\n\t"
    "s_mov_b32 s11, 0x12000\n\t"
    "buffer_load_dwordx4 v[48:51], v20, s[52:55], s11 offen sc1\n\t"
    "v_exp_f32 v134, v134\n\t"
    "v_exp_f32 v135, v135\n\t"
    "v_exp_f32 v136, v136\n\t"
    "v_exp_f32 v137, v137\n\t"
    "s_waitcnt lgkmcnt(6)\n\t"
    "v_smfmac_f32_32x32x32_bf16 v[160:175], v[180:183], v[128:135], v206\n\t"
    "s_mov_b64 exec, s[18:19]\n\t"
    "ds_read_b128 v[180:183], v205 offset:35840\n\t"
    "ds_read_b128 v[188:191], v205 offset:36352\n\t"
    "s_mov_b64 exec, -1\n\t"
    "v_exp_f32 v138, v138\n\t"
    "v_exp_f32 v139, v139\n\t"
    "v_exp_f32 v140, v140\n\t"
    "s_waitcnt lgkmcnt(5)\n\t"
    "v_mfma_f32_32x32x16_bf16 v[64:79], v[24:27], v[56:59], v[64:79]\n\t"
    "v_exp_f32 v141, v141\n\t"
    "v_exp_f32 v142, v142\n\t"
    "s_mov_b32 s11, 0x36000\n\t"
    "buffer_load_dwordx4 v[52:55], v20, s[52:55], s11 offen sc1\n\t"
    "v_exp_f32 v143, v143\n\t"
    "s_add_u32 m0, s27, 0x400\n\t"
    "s_mov_b32 s4, 0x2000\n\t"
    "buffer_load_dwordx4 v207, s[60:63], s4 offen lds\n\t"
    "v_exp_f32 v32, v32\n\t"
    "v_exp_f32 v33, v33\n\t"
    "v_smfmac_f32_32x32x32_bf16 v[160:175], v[184:187], v[136:143], v206\n\t"
    "v_exp_f32 v34, v34\n\t"
    "v_exp_f32 v35, v35\n\t"
    "v_exp_f32 v36, v36\n\t"
    "s_waitcnt lgkmcnt(2)\n\t"
    "v_mfma_f32_32x32x16_bf16 v[128:143], v[176:179], v[192:195], v[144:159]\n\t"
    "ds_read_b128 v[176:179], v204 offset:37888\n\t"
    "v_exp_f32 v37, v37\n\t"
    "v_exp_f32 v38, v38\n\t"
    "v_exp_f32 v39, v39\n\t"
    "v_exp_f32 v40, v40\n\t"
    "v_exp_f32 v41, v41\n\t"
    "s_waitcnt lgkmcnt(2)\n\t"
    "v_smfmac_f32_32x32x32_bf16 v[160:175], v[180:183], v[32:39], v206\n\t"
    "s_mov_b64 exec, s[18:19]\n\t"
    "ds_read_b128 v[180:183], v205 offset:36864\n\t"
    "ds_read_b128 v[184:187], v205 offset:37376\n\t"
    "s_mov_b64 exec, -1\n\t"
    "v_exp_f32 v42, v42\n\t"
    "v_exp_f32 v43, v43\n\t"
    "v_exp_f32 v44, v44\n\t"
    "v_mfma_f32_32x32x16_bf16 v[80:95], v[28:31], v[56:59], v[80:95]\n\t"
    "v_exp_f32 v45, v45\n\t"
    "s_add_u32 m0, s15, 0x800\n\t"
    "s_mov_b32 s4, 0x120000\n\t"
    "buffer_load_dwordx4 v207, s[56:59], s4 offen lds\n\t"
    "v_exp_f32 v46, v46\n\t"
    "v_exp_f32 v47, v47\n\t"
    "s_add_u32 m0, s27, 0x800\n\t"
    "s_mov_b32 s4, 0x20000\n\t"
    "buffer_load_dwordx4 v207, s[60:63], s4 offen lds\n\t"
    "v_exp_f32 v128, v128\n\t"
    "v_exp_f32 v129, v129\n\t"
    "s_waitcnt lgkmcnt(3)\n\t"
    "v_smfmac_f32_32x32x32_bf16 v[160:175], v[188:191], v[40:47], v206\n\t"
    "v_exp_f32 v130, v130\n\t"
    "v_exp_f32 v131, v131\n\t"
    "s_add_u32 m0, s15, 0xc00\n\t"
    "s_mov_b32 s4, 0x122000\n\t"
    "buffer_load_dwordx4 v207, s[56:59], s4 offen lds\n\t"
    "v_exp_f32 v132, v132\n\t"
    "s_waitcnt lgkmcnt(2)\n\t"
    "v_mfma_f32_32x32x16_bf16 v[32:47], v[176:179], v[192:195], v[144:159]\n\t"
    "ds_read_b128 v[176:179], v204 offset:38912\n\t"
    "v_exp_f32 v133, v133\n\t"
    "v_exp_f32 v134, v134\n\t"
    "v_exp_f32 v135, v135\n\t"
    "v_exp_f32 v136, v136\n\t"
    "v_exp_f32 v137, v137\n\t"
    "s_waitcnt lgkmcnt(2)\n\t"
    "v_smfmac_f32_32x32x32_bf16 v[160:175], v[180:183], v[128:135], v206\n\t"
    "s_mov_b64 exec, s[18:19]\n\t"
    "ds_read_b128 v[180:183], v205 offset:37888\n\t"
    "ds_read_b128 v[188:191], v205 offset:38400\n\t"
    "s_mov_b64 exec, -1\n\t"
    "v_exp_f32 v138, v138\n\t"
    "v_exp_f32 v139, v139\n\t"
    "v_exp_f32 v140, v140\n\t"
    "v_mfma_f32_32x32x16_bf16 v[96:111], v[24:27], v[60:63], v[96:111]\n\t"
    "v_exp_f32 v141, v141\n\t"
    "v_exp_f32 v142, v142\n\t"
    "v_exp_f32 v143, v143\n\t"
    "s_add_u32 m0, s27, 0xc00\n\t"
    "s_mov_b32 s4, 0x22000\n\t"
    "buffer_load_dwordx4 v207, s[60:63], s4 offen lds\n\t"
    "v_exp_f32 v32, v32\n\t"
    "v_exp_f32 v33, v33\n\t"
    "s_waitcnt lgkmcnt(3)\n\t"
    "v_smfmac_f32_32x32x32_bf16 v[160:175], v[184:187], v[136:143], v206\n\t"
    "v_exp_f32 v34, v34\n\t"
    "v_exp_f32 v35, v35\n\t"
    "v_exp_f32 v36, v36\n\t"
    "s_waitcnt lgkmcnt(2)\n\t"
    "v_mfma_f32_32x32x16_bf16 v[128:143], v[176:179], v[192:195], v[144:159]\n\t"
    "ds_read_b128 v[176:179], v204 offset:39936\n\t"
    "v_exp_f32 v37, v37\n\t"
    "v_exp_f32 v38, v38\n\t"
    "v_exp_f32 v39, v39\n\t"
    "v_exp_f32 v40, v40\n\t"
    "v_exp_f32 v41, v41\n\t"
    "s_waitcnt lgkmcnt(2)\n\t"
    "v_smfmac_f32_32x32x32_bf16 v[160:175], v[180:183], v[32:39], v206\n\t"
    "s_mov_b64 exec, s[18:19]\n\t"
    "ds_read_b128 v[180:183], v205 offset:38912\n\t"
    "ds_read_b128 v[184:187], v205 offset:39424\n\t"
    "s_mov_b64 exec, -1\n\t"
    "v_exp_f32 v42, v42\n\t"
    "v_exp_f32 v43, v43\n\t"
    "v_exp_f32 v44, v44\n\t"
    "v_mfma_f32_32x32x16_bf16 v[112:127], v[28:31], v[60:63], v[112:127]\n\t"
    "v_exp_f32 v45, v45\n\t"
    "v_exp_f32 v46, v46\n\t"
    "v_exp_f32 v47, v47\n\t"
    "s_add_u32 m0, s27, 0x1000\n\t"
    "s_mov_b32 s4, 0x40000\n\t"
    "buffer_load_dwordx4 v207, s[60:63], s4 offen lds\n\t"
    "v_exp_f32 v128, v128\n\t"
    "v_exp_f32 v129, v129\n\t"
    "s_waitcnt lgkmcnt(3)\n\t"
    "v_smfmac_f32_32x32x32_bf16 v[160:175], v[188:191], v[40:47], v206\n\t"
    "v_exp_f32 v130, v130\n\t"
    "s_waitcnt vmcnt(11)\n\t"
    "ds_read_b64_tr_b16 v[24:25], v252\n\t"
    "ds_read_b64_tr_b16 v[26:27], v252 offset:512\n\t"
    "ds_read_b64_tr_b16 v[28:29], v253\n\t"
    "ds_read_b64_tr_b16 v[30:31], v253 offset:512\n\t"
    "v_exp_f32 v131, v131\n\t"
    "v_exp_f32 v132, v132\n\t"
    "s_waitcnt lgkmcnt(6)\n\t"
    "v_mfma_f32_32x32x16_bf16 v[32:47], v[176:179], v[192:195], v[144:159]\n\t"
    "ds_read_b128 v[176:179], v204 offset:40960\n\t"
    "v_exp_f32 v133, v133\n\t"
    "s_mov_b32 s11, 0x14000\n\t"
    "buffer_load_dwordx4 v[56:59], v20, s[52:55], s11 offen sc1\n\t"
    "v_exp_f32 v134, v134\n\t"
    "v_exp_f32 v135, v135\n\t"
    "v_exp_f32 v136, v136\n\t"
    "v_exp_f32 v137, v137\n\t"
    "s_waitcnt lgkmcnt(6)\n\t"
    "v_smfmac_f32_32x32x32_bf16 v[160:175], v[180:183], v[128:135], v206\n\t"
    "s_mov_b64 exec, s[18:19]\n\t"
    "ds_read_b128 v[180:183], v205 offset:39936\n\t"
    "ds_read_b128 v[188:191], v205 offset:40448\n\t"
    "s_mov_b64 exec, -1\n\t"
    "v_exp_f32 v138, v138\n\t"
    "v_exp_f32 v139, v139\n\t"
    "v_exp_f32 v140, v140\n\t"
    "s_waitcnt lgkmcnt(5)\n\t"
    "v_mfma_f32_32x32x16_bf16 v[64:79], v[24:27], v[2:5], v[64:79]\n\t"
    "v_exp_f32 v141, v141\n\t"
    "v_exp_f32 v142, v142\n\t"
    "s_mov_b32 s11, 0x38000\n\t"
    "buffer_load_dwordx4 v[60:63], v20, s[52:55], s11 offen sc1\n\t"
    "v_exp_f32 v143, v143\n\t"
    "s_add_u32 m0, s27, 0x1400\n\t"
    "s_mov_b32 s4, 0x42000\n\t"
    "buffer_load_dwordx4 v207, s[60:63], s4 offen lds\n\t"
    "v_exp_f32 v32, v32\n\t"
    "v_exp_f32 v33, v33\n\t"
    "v_smfmac_f32_32x32x32_bf16 v[160:175], v[184:187], v[136:143], v206\n\t"
    "v_exp_f32 v34, v34\n\t"
    "v_exp_f32 v35, v35\n\t"
    "v_exp_f32 v36, v36\n\t"
    "s_waitcnt lgkmcnt(2)\n\t"
    "v_mfma_f32_32x32x16_bf16 v[128:143], v[176:179], v[192:195], v[144:159]\n\t"
    "ds_read_b128 v[176:179], v204 offset:41984\n\t"
    "v_exp_f32 v37, v37\n\t"
    "v_exp_f32 v38, v38\n\t"
    "v_exp_f32 v39, v39\n\t"
    "v_exp_f32 v40, v40\n\t"
    "v_exp_f32 v41, v41\n\t"
    "s_waitcnt lgkmcnt(2)\n\t"
    "v_smfmac_f32_32x32x32_bf16 v[160:175], v[180:183], v[32:39], v206\n\t"
    "s_mov_b64 exec, s[18:19]\n\t"
    "ds_read_b128 v[180:183], v205 offset:40960\n\t"
    "ds_read_b128 v[184:187], v205 offset:41472\n\t"
    "s_mov_b64 exec, -1\n\t"
    "v_exp_f32 v42, v42\n\t"
    "v_exp_f32 v43, v43\n\t"
    "v_exp_f32 v44, v44\n\t"
    "v_mfma_f32_32x32x16_bf16 v[80:95], v[28:31], v[2:5], v[80:95]\n\t"
    "v_exp_f32 v45, v45\n\t"
    "s_add_u32 m0, s15, 0\n\t"
    "s_mov_b32 s4, 0x140000\n\t"
    "buffer_load_dwordx4 v207, s[56:59], s4 offen lds\n\t"
    "v_exp_f32 v46, v46\n\t"
    "v_exp_f32 v47, v47\n\t"
    "s_add_u32 m0, s27, 0x1800\n\t"
    "s_mov_b32 s4, 0x60000\n\t"
    "buffer_load_dwordx4 v207, s[60:63], s4 offen lds\n\t"
    "v_exp_f32 v128, v128\n\t"
    "v_exp_f32 v129, v129\n\t"
    "s_waitcnt lgkmcnt(3)\n\t"
    "v_smfmac_f32_32x32x32_bf16 v[160:175], v[188:191], v[40:47], v206\n\t"
    "v_exp_f32 v130, v130\n\t"
    "v_exp_f32 v131, v131\n\t"
    "s_add_u32 m0, s15, 0x400\n\t"
    "s_mov_b32 s4, 0x142000\n\t"
    "buffer_load_dwordx4 v207, s[56:59], s4 offen lds\n\t"
    "v_exp_f32 v132, v132\n\t"
    "s_waitcnt lgkmcnt(2)\n\t"
    "v_mfma_f32_32x32x16_bf16 v[32:47], v[176:179], v[192:195], v[144:159]\n\t"
    "ds_read_b128 v[176:179], v204 offset:43008\n\t"
    "v_exp_f32 v133, v133\n\t"
    "v_exp_f32 v134, v134\n\t"
    "v_exp_f32 v135, v135\n\t"
    "v_exp_f32 v136, v136\n\t"
    "v_exp_f32 v137, v137\n\t"
    "s_waitcnt lgkmcnt(2)\n\t"
    "v_smfmac_f32_32x32x32_bf16 v[160:175], v[180:183], v[128:135], v206\n\t"
    "s_mov_b64 exec, s[18:19]\n\t"
    "ds_read_b128 v[180:183], v205 offset:41984\n\t"
    "ds_read_b128 v[188:191], v205 offset:42496\n\t"
    "s_mov_b64 exec, -1\n\t"
    "v_exp_f32 v138, v138\n\t"
    "v_exp_f32 v139, v139\n\t"
    "v_exp_f32 v140, v140\n\t"
    "v_mfma_f32_32x32x16_bf16 v[96:111], v[24:27], v[6:9], v[96:111]\n\t"
    "v_exp_f32 v141, v141\n\t"
    "v_exp_f32 v142, v142\n\t"
    "v_exp_f32 v143, v143\n\t"
    "s_add_u32 m0, s27, 0x1c00\n\t"
    "s_mov_b32 s4, 0x62000\n\t"
    "buffer_load_dwordx4 v207, s[60:63], s4 offen lds\n\t"
    "v_exp_f32 v32, v32\n\t"
    "v_exp_f32 v33, v33\n\t"
    "s_waitcnt lgkmcnt(3)\n\t"
    "v_smfmac_f32_32x32x32_bf16 v[160:175], v[184:187], v[136:143], v206\n\t"
    "v_exp_f32 v34, v34\n\t"
    "v_exp_f32 v35, v35\n\t"
    "v_exp_f32 v36, v36\n\t"
    "s_waitcnt lgkmcnt(2)\n\t"
    "v_mfma_f32_32x32x16_bf16 v[128:143], v[176:179], v[192:195], v[144:159]\n\t"
    "ds_read_b128 v[176:179], v204 offset:44032\n\t"
    "v_exp_f32 v37, v37\n\t"
    "v_exp_f32 v38, v38\n\t"
    "v_exp_f32 v39, v39\n\t"
    "v_exp_f32 v40, v40\n\t"
    "v_exp_f32 v41, v41\n\t"
    "s_waitcnt lgkmcnt(2)\n\t"
    "v_smfmac_f32_32x32x32_bf16 v[160:175], v[180:183], v[32:39], v206\n\t"
    "s_mov_b64 exec, s[18:19]\n\t"
    "ds_read_b128 v[180:183], v205 offset:43008\n\t"
    "ds_read_b128 v[184:187], v205 offset:43520\n\t"
    "s_mov_b64 exec, -1\n\t"
    "v_exp_f32 v42, v42\n\t"
    "v_exp_f32 v43, v43\n\t"
    "v_exp_f32 v44, v44\n\t"
    "v_mfma_f32_32x32x16_bf16 v[112:127], v[28:31], v[6:9], v[112:127]\n\t"
    "v_exp_f32 v45, v45\n\t"
    "v_exp_f32 v46, v46\n\t"
    "v_exp_f32 v47, v47\n\t"
    "s_add_u32 m0, s29, 0\n\t"
    "s_mov_b32 s4, 0x180000\n\t"
    "buffer_load_dwordx4 v207, s[56:59], s4 offen lds\n\t"
    "v_exp_f32 v128, v128\n\t"
    "v_exp_f32 v129, v129\n\t"
    "s_waitcnt lgkmcnt(3)\n\t"
    "v_smfmac_f32_32x32x32_bf16 v[160:175], v[188:191], v[40:47], v206\n\t"
    "v_exp_f32 v130, v130\n\t"
    "s_waitcnt vmcnt(10)\n\t"
    "ds_read_b64_tr_b16 v[24:25], v252 offset:2048\n\t"
    "ds_read_b64_tr_b16 v[26:27], v252 offset:2560\n\t"
    "ds_read_b64_tr_b16 v[28:29], v253 offset:2048\n\t"
    "ds_read_b64_tr_b16 v[30:31], v253 offset:2560\n\t"
    "v_exp_f32 v131, v131\n\t"
    "v_exp_f32 v132, v132\n\t"
    "s_waitcnt lgkmcnt(6)\n\t"
    "v_mfma_f32_32x32x16_bf16 v[32:47], v[176:179], v[192:195], v[144:159]\n\t"
    "ds_read_b128 v[176:179], v204 offset:45056\n\t"
    "v_exp_f32 v133, v133\n\t"
    "s_mov_b32 s11, 0x16000\n\t"
    "buffer_load_dwordx4 v[2:5], v20, s[52:55], s11 offen sc1\n\t"
    "v_exp_f32 v134, v134\n\t"
    "s_cmp_ge_u32 s8, 4\n\t"
    "s_cbranch_scc1 L_pr5_%=\n\t"
    "s_setprio 0\n\t"
    "L_pr5_%=:\n\t"
    "v_exp_f32 v135, v135\n\t"
    "v_exp_f32 v136, v136\n\t"
    "v_exp_f32 v137, v137\n\t"
    "s_waitcnt lgkmcnt(6)\n\t"
    "v_smfmac_f32_32x32x32_bf16 v[160:175], v[180:183], v[128:135], v206\n\t"
    "s_mov_b64 exec, s[18:19]\n\t"
    "ds_read_b128 v[180:183], v205 offset:44032\n\t"
    "ds_read_b128 v[188:191], v205 offset:44544\n\t"
    "s_mov_b64 exec, -1\n\t"
    "v_exp_f32 v138, v138\n\t"
    "v_exp_f32 v139, v139\n\t"
    "v_exp_f32 v140, v140\n\t"
    "s_waitcnt lgkmcnt(5)\n\t"
    "v_mfma_f32_32x32x16_bf16 v[64:79], v[24:27], v[48:51], v[64:79]\n\t"
    "v_exp_f32 v141, v141\n\t"
    "v_exp_f32 v142, v142\n\t"
    "s_mov_b32 s11, 0x3a000\n\t"
    "buffer_load_dwordx4 v[6:9], v20, s[52:55], s11 offen sc1\n\t"
    "v_exp_f32 v143, v143\n\t"
    "s_add_u32 m0, s29, 0x400\n\t"
    "s_mov_b32 s4, 0x182000\n\t"
    "buffer_load_dwordx4 v207, s[56:59], s4 offen lds\n\t"
    "v_exp_f32 v32, v32\n\t"
    "v_exp_f32 v33, v33\n\t"
    "v_smfmac_f32_32x32x32_bf16 v[160:175], v[184:187], v[136:143], v206\n\t"
    "v_exp_f32 v34, v34\n\t"
    "v_exp_f32 v35, v35\n\t"
    "v_exp_f32 v36, v36\n\t"
    "s_waitcnt lgkmcnt(2)\n\t"
    "v_mfma_f32_32x32x16_bf16 v[128:143], v[176:179], v[192:195], v[144:159]\n\t"
    "ds_read_b128 v[176:179], v204 offset:46080\n\t"
    "v_exp_f32 v37, v37\n\t"
    "v_exp_f32 v38, v38\n\t"
    "v_exp_f32 v39, v39\n\t"
    "v_exp_f32 v40, v40\n\t"
    "v_exp_f32 v41, v41\n\t"
    "s_waitcnt lgkmcnt(2)\n\t"
    "v_smfmac_f32_32x32x32_bf16 v[160:175], v[180:183], v[32:39], v206\n\t"
    "s_mov_b64 exec, s[18:19]\n\t"
    "ds_read_b128 v[180:183], v205 offset:45056\n\t"
    "ds_read_b128 v[184:187], v205 offset:45568\n\t"
    "s_mov_b64 exec, -1\n\t"
    "v_exp_f32 v42, v42\n\t"
    "v_exp_f32 v43, v43\n\t"
    "v_exp_f32 v44, v44\n\t"
    "v_mfma_f32_32x32x16_bf16 v[80:95], v[28:31], v[48:51], v[80:95]\n\t"
    "v_exp_f32 v45, v45\n\t"
    "s_add_u32 m0, s15, 0x800\n\t"
    "s_mov_b32 s4, 0x160000\n\t"
    "buffer_load_dwordx4 v207, s[56:59], s4 offen lds\n\t"
    "v_exp_f32 v46, v46\n\t"
    "v_exp_f32 v47, v47\n\t"
    "s_add_u32 m0, s29, 0x800\n\t"
    "s_mov_b32 s4, 0x1a0000\n\t"
    "buffer_load_dwordx4 v207, s[56:59], s4 offen lds\n\t"
    "v_exp_f32 v128, v128\n\t"
    "v_exp_f32 v129, v129\n\t"
    "s_waitcnt lgkmcnt(3)\n\t"
    "v_smfmac_f32_32x32x32_bf16 v[160:175], v[188:191], v[40:47], v206\n\t"
    "v_exp_f32 v130, v130\n\t"
    "v_exp_f32 v131, v131\n\t"
    "s_add_u32 m0, s15, 0xc00\n\t"
    "s_mov_b32 s4, 0x162000\n\t"
    "buffer_load_dwordx4 v207, s[56:59], s4 offen lds\n\t"
    "v_exp_f32 v132, v132\n\t"
    "s_waitcnt lgkmcnt(2)\n\t"
    "v_mfma_f32_32x32x16_bf16 v[32:47], v[176:179], v[192:195], v[144:159]\n\t"
    "ds_read_b128 v[176:179], v204 offset:47104\n\t"
    "v_exp_f32 v133, v133\n\t"
    "s_mov_b32 s11, 0x1a000\n\t"
    "buffer_load_dwordx4 v[10:13], v20, s[52:55], s11 offen sc1\n\t"
    "v_exp_f32 v134, v134\n\t"
    "v_exp_f32 v135, v135\n\t"
    "v_exp_f32 v136, v136\n\t"
    "v_exp_f32 v137, v137\n\t"
    "s_waitcnt lgkmcnt(2)\n\t"
    "v_smfmac_f32_32x32x32_bf16 v[160:175], v[180:183], v[128:135], v206\n\t"
    "s_mov_b64 exec, s[18:19]\n\t"
    "ds_read_b128 v[180:183], v205 offset:46080\n\t"
    "ds_read_b128 v[188:191], v205 offset:46592\n\t"
    "s_mov_b64 exec, -1\n\t"
    "v_exp_f32 v138, v138\n\t"
    "v_exp_f32 v139, v139\n\t"
    "v_exp_f32 v140, v140\n\t"
    "v_mfma_f32_32x32x16_bf16 v[96:111], v[24:27], v[52:55], v[96:111]\n\t"
    "v_exp_f32 v141, v141\n\t"
    "v_exp_f32 v142, v142\n\t"
    "s_mov_b32 s11, 0x3e000\n\t"
    "buffer_load_dwordx4 v[14:17], v20, s[52:55], s11 offen sc1\n\t"
    "v_exp_f32 v143, v143\n\t"
    "s_add_u32 m0, s29, 0xc00\n\t"
    "s_mov_b32 s4, 0x1a2000\n\t"
    "buffer_load_dwordx4 v207, s[56:59], s4 offen lds\n\t"
    "v_exp_f32 v32, v32\n\t"
    "v_exp_f32 v33, v33\n\t"
    "s_waitcnt lgkmcnt(3)\n\t"
    "v_smfmac_f32_32x32x32_bf16 v[160:175], v[184:187], v[136:143], v206\n\t"
    "v_exp_f32 v34, v34\n\t"
    "v_exp_f32 v35, v35\n\t"
    "v_exp_f32 v36, v36\n\t"
    "s_waitcnt lgkmcnt(2)\n\t"
    "v_mfma_f32_32x32x16_bf16 v[128:143], v[176:179], v[192:195], v[144:159]\n\t"
    "ds_read_b128 v[176:179], v204 offset:48128\n\t"
    "v_exp_f32 v37, v37\n\t"
    "v_exp_f32 v38, v38\n\t"
    "v_exp_f32 v39, v39\n\t"
    "v_exp_f32 v40, v40\n\t"
    "v_exp_f32 v41, v41\n\t"
    "s_waitcnt lgkmcnt(2)\n\t"
    "v_smfmac_f32_32x32x32_bf16 v[160:175], v[180:183], v[32:39], v206\n\t"
    "s_mov_b64 exec, s[18:19]\n\t"
    "ds_read_b128 v[180:183], v205 offset:47104\n\t"
    "ds_read_b128 v[184:187], v205 offset:47616\n\t"
    "s_mov_b64 exec, -1\n\t"
    "v_exp_f32 v42, v42\n\t"
    "v_exp_f32 v43, v43\n\t"
    "v_exp_f32 v44, v44\n\t"
    "v_mfma_f32_32x32x16_bf16 v[112:127], v[28:31], v[52:55], v[112:127]\n\t"
    "v_exp_f32 v45, v45\n\t"
    "v_exp_f32 v46, v46\n\t"
    "v_exp_f32 v47, v47\n\t"
    "v_exp_f32 v128, v128\n\t"
    "v_exp_f32 v129, v129\n\t"
    "s_waitcnt lgkmcnt(3)\n\t"
    "v_smfmac_f32_32x32x32_bf16 v[160:175], v[188:191], v[40:47], v206\n\t"
    "v_exp_f32 v130, v130\n\t"
    "v_exp_f32 v131, v131\n\t"
    "v_exp_f32 v132, v132\n\t"
    "s_waitcnt lgkmcnt(2)\n\t"
    "v_mfma_f32_32x32x16_bf16 v[32:47], v[176:179], v[192:195], v[144:159]\n\t"
    "v_exp_f32 v133, v133\n\t"
    "v_exp_f32 v134, v134\n\t"
    "v_exp_f32 v135, v135\n\t"
    "v_exp_f32 v136, v136\n\t"
    "v_exp_f32 v137, v137\n\t"
    "s_waitcnt lgkmcnt(1)\n\t"
    "v_smfmac_f32_32x32x32_bf16 v[160:175], v[180:183], v[128:135], v206\n\t"
    "s_mov_b64 exec, s[18:19]\n\t"
    "ds_read_b128 v[180:183], v205 offset:48128\n\t"
    "ds_read_b128 v[188:191], v205 offset:48640\n\t"
    "s_mov_b64 exec, -1\n\t"
    "v_exp_f32 v138, v138\n\t"
    "v_exp_f32 v139, v139\n\t"
    "v_exp_f32 v140, v140\n\t"
    "v_exp_f32 v141, v141\n\t"
    "s_mov_b32 s11, 0x18000\n\t"
    "buffer_load_dwordx4 v[48:51], v20, s[52:55], s11 offen sc1\n\t"
    "s_mov_b32 s11, 0x3c000\n\t"
    "buffer_load_dwordx4 v[52:55], v20, s[52:55], s11 offen sc1\n\t"
    "v_exp_f32 v142, v142\n\t"
    "v_exp_f32 v143, v143\n\t"
    "v_exp_f32 v32, v32\n\t"
    "v_exp_f32 v33, v33\n\t"
    "s_waitcnt lgkmcnt(2)\n\t"
    "v_smfmac_f32_32x32x32_bf16 v[160:175], v[184:187], v[136:143], v206\n\t"
    "v_exp_f32 v34, v34\n\t"
    "v_exp_f32 v35, v35\n\t"
    "v_exp_f32 v36, v36\n\t"
    "v_exp_f32 v37, v37\n\t"
    "v_exp_f32 v38, v38\n\t"
    "v_exp_f32 v39, v39\n\t"
    "v_exp_f32 v40, v40\n\t"
    "v_exp_f32 v41, v41\n\t"
    "s_waitcnt lgkmcnt(1)\n\t"
    "v_smfmac_f32_32x32x32_bf16 v[160:175], v[180:183], v[32:39], v206\n\t"
    "v_exp_f32 v42, v42\n\t"
    "v_exp_f32 v43, v43\n\t"
    "v_exp_f32 v44, v44\n\t"
    "v_exp_f32 v45, v45\n\t"
    "v_exp_f32 v46, v46\n\t"
    "v_exp_f32 v47, v47\n\t"
    "s_nop 1\n\t"
    "s_waitcnt lgkmcnt(0)\n\t"
    "v_smfmac_f32_32x32x32_bf16 v[160:175], v[188:191], v[40:47], v206\n\t"
    "s_waitcnt vmcnt(2)\n\t"
    "ds_read_b64_tr_b16 v[24:25], v252\n\t"
    "ds_read_b64_tr_b16 v[26:27], v252 offset:512\n\t"
    "ds_read_b64_tr_b16 v[28:29], v253\n\t"
    "ds_read_b64_tr_b16 v[30:31], v253 offset:512\n\t"
    "v_mov_b32 v209, 0x207\n\t"
    "s_nop 5\n\t"
    "v_cmp_class_f32_e64 s[74:75], v160, v209\n\t"
    "v_cmp_class_f32_e64 s[76:77], v161, v209\n\t"
    "v_cmp_class_f32_e64 s[78:79], v162, v209\n\t"
    "v_cmp_class_f32_e64 s[80:81], v163, v209\n\t"
    "s_mov_b64 s[24:25], s[74:75]\n\t"
    "v_cmp_class_f32_e64 s[74:75], v164, v209\n\t"
    "s_or_b64 s[24:25], s[24:25], s[76:77]\n\t"
    "v_cmp_class_f32_e64 s[76:77], v165, v209\n\t"
    "s_or_b64 s[24:25], s[24:25], s[78:79]\n\t"
    "v_cmp_class_f32_e64 s[78:79], v166, v209\n\t"
    "s_or_b64 s[24:25], s[24:25], s[80:81]\n\t"
    "v_cmp_class_f32_e64 s[80:81], v167, v209\n\t"
    "v_cmp_class_f32_e64 vcc, v168, v209\n\t"
    "s_or_b64 s[24:25], s[24:25], s[74:75]\n\t"
    "s_or_b64 s[24:25], s[24:25], s[76:77]\n\t"
    "s_or_b64 s[24:25], s[24:25], s[78:79]\n\t"
    "s_or_b64 s[24:25], s[24:25], s[80:81]\n\t"
    "s_or_b64 s[24:25], s[24:25], vcc\n\t"
    "s_cbranch_scc1 L_fb_%=\n\t"
    "L_fb_done_%=:\n\t"
    "v_rcp_f32 v208, v168\n\t"
    "s_nop 0\n\t"
    "v_mul_f32 v208, v208, v196\n\t"
    "v_mul_f32 v160, v160, v208\n\t"
    "v_mul_f32 v161, v161, v208\n\t"
    "v_mul_f32 v162, v162, v208\n\t"
    "v_mul_f32 v163, v163, v208\n\t"
    "v_mul_f32 v164, v164, v208\n\t"
    "v_mul_f32 v165, v165, v208\n\t"
    "v_mul_f32 v166, v166, v208\n\t"
    "v_mul_f32 v167, v167, v208\n\t"
    "v_cvt_pk_bf16_f32 v212, v160, v161\n\t"
    "v_cvt_pk_bf16_f32 v213, v162, v163\n\t"
    "v_cvt_pk_bf16_f32 v214, v164, v165\n\t"
    "v_cvt_pk_bf16_f32 v215, v166, v167\n\t"
    "v_min_u32 v212, -2, v212\n\t"
    "v_min_u32 v213, -2, v213\n\t"
    "v_min_u32 v214, -2, v214\n\t"
    "v_min_u32 v215, -2, v215\n\t"
    "buffer_store_dwordx4 v[212:215], v19, s[52:55], 0 offen sc1\n\t"
    "ds_read_b64_tr_b16 v[144:145], v252 offset:2048\n\t"
    "ds_read_b64_tr_b16 v[146:147], v252 offset:2560\n\t"
    "ds_read_b64_tr_b16 v[148:149], v253 offset:2048\n\t"
    "ds_read_b64_tr_b16 v[150:151], v253 offset:2560\n\t"
    "s_sub_u32 s17, s29, s15\n\t"
    "v_add_u32 v252, s17, v252\n\t"
    "v_add_u32 v253, s17, v253\n\t"
    "ds_read_b64_tr_b16 v[152:153], v252\n\t"
    "ds_read_b64_tr_b16 v[154:155], v252 offset:512\n\t"
    "ds_read_b64_tr_b16 v[156:157], v253\n\t"
    "ds_read_b64_tr_b16 v[158:159], v253 offset:512\n\t"
    "ds_read_b64_tr_b16 v[176:177], v252 offset:2048\n\t"
    "ds_read_b64_tr_b16 v[178:179], v252 offset:2560\n\t"
    "ds_read_b64_tr_b16 v[180:181], v253 offset:2048\n\t"
    "ds_read_b64_tr_b16 v[182:183], v253 offset:2560\n\t"
    "s_sub_u32 s17, s27, s29\n\t"
    "v_add_u32 v252, s17, v252\n\t"
    "v_add_u32 v253, s17, v253\n\t"
    "v_lshrrev_b32 v208, 5, v1\n\t"
    "v_lshlrev_b32 v208, 9, v208\n\t"
    "v_sub_u32 v252, v252, v208\n\t"
    "v_sub_u32 v253, v253, v208\n\t"
    "ds_read_b64_tr_b16 v[32:33], v252\n\t"
    "ds_read_b64_tr_b16 v[34:35], v252 offset:1024\n\t"
    "ds_read_b64_tr_b16 v[36:37], v253\n\t"
    "ds_read_b64_tr_b16 v[38:39], v253 offset:1024\n\t"
    "ds_read_b64_tr_b16 v[40:41], v252 offset:2048\n\t"
    "ds_read_b64_tr_b16 v[42:43], v252 offset:3072\n\t"
    "ds_read_b64_tr_b16 v[44:45], v253 offset:2048\n\t"
    "ds_read_b64_tr_b16 v[46:47], v253 offset:3072\n\t"
    "ds_read_b64_tr_b16 v[128:129], v252 offset:4096\n\t"
    "ds_read_b64_tr_b16 v[130:131], v252 offset:5120\n\t"
    "ds_read_b64_tr_b16 v[132:133], v253 offset:4096\n\t"
    "ds_read_b64_tr_b16 v[134:135], v253 offset:5120\n\t"
    "ds_read_b64_tr_b16 v[136:137], v252 offset:6144\n\t"
    "ds_read_b64_tr_b16 v[138:139], v252 offset:7168\n\t"
    "ds_read_b64_tr_b16 v[140:141], v253 offset:6144\n\t"
    "ds_read_b64_tr_b16 v[142:143], v253 offset:7168\n\t"
    "s_lshl_b32 s11, s8, 11\n\t"
    "v_add_u32 v22, s11, v203\n\t"
    "v_add_u32 v23, 0x10000, v22\n\t"
    "s_lshl_b32 s11, s8, 14\n\t"
    "v_add_u32 v18, s11, v203\n\t"
    "s_waitcnt lgkmcnt(0)\n\t"
    "s_barrier\n\t"
    "v_mfma_f32_32x32x16_bf16 v[64:79], v[24:27], v[56:59], v[64:79]\n\t"
    "v_mfma_f32_32x32x16_bf16 v[80:95], v[28:31], v[56:59], v[80:95]\n\t"
    "v_mfma_f32_32x32x16_bf16 v[96:111], v[24:27], v[60:63], v[96:111]\n\t"
    "v_mfma_f32_32x32x16_bf16 v[112:127], v[28:31], v[60:63], v[112:127]\n\t"
    "v_mfma_f32_32x32x16_bf16 v[64:79], v[144:147], v[2:5], v[64:79]\n\t"
    "v_mfma_f32_32x32x16_bf16 v[80:95], v[148:151], v[2:5], v[80:95]\n\t"
    "v_mfma_f32_32x32x16_bf16 v[96:111], v[144:147], v[6:9], v[96:111]\n\t"
    "v_mfma_f32_32x32x16_bf16 v[112:127], v[148:151], v[6:9], v[112:127]\n\t"
    "s_waitcnt vmcnt(2)\n\t"
    "v_mfma_f32_32x32x16_bf16 v[64:79], v[152:155], v[48:51], v[64:79]\n\t"
    "v_mfma_f32_32x32x16_bf16 v[80:95], v[156:159], v[48:51], v[80:95]\n\t"
    "s_waitcnt vmcnt(1)\n\t"
    "v_mfma_f32_32x32x16_bf16 v[96:111], v[152:155], v[52:55], v[96:111]\n\t"
    "v_mfma_f32_32x32x16_bf16 v[112:127], v[156:159], v[52:55], v[112:127]\n\t"
    "v_mfma_f32_32x32x16_bf16 v[64:79], v[176:179], v[10:13], v[64:79]\n\t"
    "v_mfma_f32_32x32x16_bf16 v[80:95], v[180:183], v[10:13], v[80:95]\n\t"
    "v_mfma_f32_32x32x16_bf16 v[96:111], v[176:179], v[14:17], v[96:111]\n\t"
    "v_mfma_f32_32x32x16_bf16 v[112:127], v[180:183], v[14:17], v[112:127]\n\t"
    "s_mov_b32 s11, 0x1c000\n\t"
    "buffer_load_dwordx4 v[216:219], v20, s[52:55], s11 offen sc1\n\t"
    "s_mov_b32 s11, 0x40000\n\t"
    "buffer_load_dwordx4 v[220:223], v20, s[52:55], s11 offen sc1\n\t"
    "s_mov_b32 s11, 0x1e000\n\t"
    "buffer_load_dwordx4 v[224:227], v20, s[52:55], s11 offen sc1\n\t"
    "s_mov_b32 s11, 0x42000\n\t"
    "buffer_load_dwordx4 v[228:231], v20, s[52:55], s11 offen sc1\n\t"
    "s_mov_b32 s11, 0x20000\n\t"
    "buffer_load_dwordx4 v[232:235], v20, s[52:55], s11 offen sc1\n\t"
    "s_mov_b32 s11, 0x44000\n\t"
    "buffer_load_dwordx4 v[236:239], v20, s[52:55], s11 offen sc1\n\t"
    "s_mov_b32 s11, 0x22000\n\t"
    "buffer_load_dwordx4 v[240:243], v20, s[52:55], s11 offen sc1\n\t"
    "s_mov_b32 s11, 0x46000\n\t"
    "buffer_load_dwordx4 v[244:247], v20, s[52:55], s11 offen sc1\n\t"
    "s_waitcnt vmcnt(7)\n\t"
    "v_max3_u32 v208, v216, v217, v218\n\t"
    "s_waitcnt vmcnt(6)\n\t"
    "v_max3_u32 v209, v219, v220, v221\n\t"
    "v_max3_u32 v208, v208, v222, v223\n\t"
    "v_max_u32 v208, v208, v209\n\t"
    "v_cmp_eq_u32 vcc, -1, v208\n\t"
    "s_nop 1\n\t"
    "s_cbranch_vccnz L_o0_retry_%=\n\t"
    "L_o0_ok_%=:\n\t"
    "v_mfma_f32_32x32x16_bf16 v[64:79], v[32:35], v[216:219], v[64:79]\n\t"
    "v_mfma_f32_32x32x16_bf16 v[80:95], v[36:39], v[216:219], v[80:95]\n\t"
    "v_mfma_f32_32x32x16_bf16 v[96:111], v[32:35], v[220:223], v[96:111]\n\t"
    "v_mfma_f32_32x32x16_bf16 v[112:127], v[36:39], v[220:223], v[112:127]\n\t"
    "s_waitcnt vmcnt(5)\n\t"
    "v_max3_u32 v208, v224, v225, v226\n\t"
    "s_waitcnt vmcnt(4)\n\t"
    "v_max3_u32 v209, v227, v228, v229\n\t"
    "v_max3_u32 v208, v208, v230, v231\n\t"
    "v_max_u32 v208, v208, v209\n\t"
    "v_cmp_eq_u32 vcc, -1, v208\n\t"
    "s_nop 1\n\t"
    "s_cbranch_vccnz L_o1_retry_%=\n\t"
    "L_o1_ok_%=:\n\t"
    "v_mfma_f32_32x32x16_bf16 v[64:79], v[40:43], v[224:227], v[64:79]\n\t"
    "v_mfma_f32_32x32x16_bf16 v[80:95], v[44:47], v[224:227], v[80:95]\n\t"
    "v_mfma_f32_32x32x16_bf16 v[96:111], v[40:43], v[228:231], v[96:111]\n\t"
    "v_mfma_f32_32x32x16_bf16 v[112:127], v[44:47], v[228:231], v[112:127]\n\t"
    "s_waitcnt vmcnt(3)\n\t"
    "v_max3_u32 v208, v232, v233, v234\n\t"
    "s_waitcnt vmcnt(2)\n\t"
    "v_max3_u32 v209, v235, v236, v237\n\t"
    "v_max3_u32 v208, v208, v238, v239\n\t"
    "v_max_u32 v208, v208, v209\n\t"
    "v_cmp_eq_u32 vcc, -1, v208\n\t"
    "s_nop 1\n\t"
    "s_cbranch_vccnz L_o2_retry_%=\n\t"
    "L_o2_ok_%=:\n\t"
    "v_mfma_f32_32x32x16_bf16 v[64:79], v[128:131], v[232:235], v[64:79]\n\t"
    "v_mfma_f32_32x32x16_bf16 v[80:95], v[132:135], v[232:235], v[80:95]\n\t"
    "v_mfma_f32_32x32x16_bf16 v[96:111], v[128:131], v[236:239], v[96:111]\n\t"
    "v_mfma_f32_32x32x16_bf16 v[112:127], v[132:135], v[236:239], v[112:127]\n\t"
    "s_waitcnt vmcnt(1)\n\t"
    "v_max3_u32 v208, v240, v241, v242\n\t"
    "s_waitcnt vmcnt(0)\n\t"
    "v_max3_u32 v209, v243, v244, v245\n\t"
    "v_max3_u32 v208, v208, v246, v247\n\t"
    "v_max_u32 v208, v208, v209\n\t"
    "v_cmp_eq_u32 vcc, -1, v208\n\t"
    "s_nop 1\n\t"
    "s_cbranch_vccnz L_o3_retry_%=\n\t"
    "L_o3_ok_%=:\n\t"
    "v_mfma_f32_32x32x16_bf16 v[64:79], v[136:139], v[240:243], v[64:79]\n\t"
    "v_mfma_f32_32x32x16_bf16 v[80:95], v[140:143], v[240:243], v[80:95]\n\t"
    "v_mfma_f32_32x32x16_bf16 v[96:111], v[136:139], v[244:247], v[96:111]\n\t"
    "v_mfma_f32_32x32x16_bf16 v[112:127], v[140:143], v[244:247], v[112:127]\n\t"
    "s_nop 8\n\t"
    "ds_write_b128 v22, v[64:67]\n\t"
    "ds_write_b128 v22, v[68:71] offset:1024\n\t"
    "ds_write_b128 v22, v[72:75] offset:16384\n\t"
    "ds_write_b128 v22, v[76:79] offset:17408\n\t"
    "ds_write_b128 v22, v[80:83] offset:32768\n\t"
    "ds_write_b128 v22, v[84:87] offset:33792\n\t"
    "ds_write_b128 v22, v[88:91] offset:49152\n\t"
    "ds_write_b128 v22, v[92:95] offset:50176\n\t"
    "ds_write_b128 v23, v[96:99]\n\t"
    "ds_write_b128 v23, v[100:103] offset:1024\n\t"
    "ds_write_b128 v23, v[104:107] offset:16384\n\t"
    "ds_write_b128 v23, v[108:111] offset:17408\n\t"
    "ds_write_b128 v23, v[112:115] offset:32768\n\t"
    "ds_write_b128 v23, v[116:119] offset:33792\n\t"
    "ds_write_b128 v23, v[120:123] offset:49152\n\t"
    "ds_write_b128 v23, v[124:127] offset:50176\n\t"
    "s_waitcnt lgkmcnt(0)\n\t"
    "s_barrier\n\t"
    "ds_read_b128 v[128:131], v18\n\t"
    "ds_read_b128 v[132:135], v18 offset:1024\n\t"
    "ds_read_b128 v[136:139], v18 offset:2048\n\t"
    "ds_read_b128 v[140:143], v18 offset:3072\n\t"
    "ds_read_b128 v[144:147], v18 offset:4096\n\t"
    "ds_read_b128 v[148:151], v18 offset:5120\n\t"
    "ds_read_b128 v[152:155], v18 offset:6144\n\t"
    "ds_read_b128 v[156:159], v18 offset:7168\n\t"
    "ds_read_b128 v[160:163], v18 offset:8192\n\t"
    "ds_read_b128 v[164:167], v18 offset:9216\n\t"
    "ds_read_b128 v[168:171], v18 offset:10240\n\t"
    "ds_read_b128 v[172:175], v18 offset:11264\n\t"
    "ds_read_b128 v[176:179], v18 offset:12288\n\t"
    "ds_read_b128 v[180:183], v18 offset:13312\n\t"
    "ds_read_b128 v[184:187], v18 offset:14336\n\t"
    "ds_read_b128 v[188:191], v18 offset:15360\n\t"
    "s_waitcnt lgkmcnt(13)\n\t"
    "v_add_f32 v128, v128, v136\n\t"
    "v_add_f32 v129, v129, v137\n\t"
    "v_add_f32 v130, v130, v138\n\t"
    "v_add_f32 v131, v131, v139\n\t"
    "s_waitcnt lgkmcnt(12)\n\t"
    "v_add_f32 v132, v132, v140\n\t"
    "v_add_f32 v133, v133, v141\n\t"
    "v_add_f32 v134, v134, v142\n\t"
    "v_add_f32 v135, v135, v143\n\t"
    "s_waitcnt lgkmcnt(9)\n\t"
    "v_add_f32 v144, v144, v152\n\t"
    "v_add_f32 v145, v145, v153\n\t"
    "v_add_f32 v146, v146, v154\n\t"
    "v_add_f32 v147, v147, v155\n\t"
    "s_waitcnt lgkmcnt(8)\n\t"
    "v_add_f32 v148, v148, v156\n\t"
    "v_add_f32 v149, v149, v157\n\t"
    "v_add_f32 v150, v150, v158\n\t"
    "v_add_f32 v151, v151, v159\n\t"
    "s_waitcnt lgkmcnt(5)\n\t"
    "v_add_f32 v160, v160, v168\n\t"
    "v_add_f32 v161, v161, v169\n\t"
    "v_add_f32 v162, v162, v170\n\t"
    "v_add_f32 v163, v163, v171\n\t"
    "s_waitcnt lgkmcnt(4)\n\t"
    "v_add_f32 v164, v164, v172\n\t"
    "v_add_f32 v165, v165, v173\n\t"
    "v_add_f32 v166, v166, v174\n\t"
    "v_add_f32 v167, v167, v175\n\t"
    "s_waitcnt lgkmcnt(1)\n\t"
    "v_add_f32 v176, v176, v184\n\t"
    "v_add_f32 v177, v177, v185\n\t"
    "v_add_f32 v178, v178, v186\n\t"
    "v_add_f32 v179, v179, v187\n\t"
    "s_waitcnt lgkmcnt(0)\n\t"
    "v_add_f32 v180, v180, v188\n\t"
    "v_add_f32 v181, v181, v189\n\t"
    "v_add_f32 v182, v182, v190\n\t"
    "v_add_f32 v183, v183, v191\n\t"
    "v_add_f32 v128, v128, v144\n\t"
    "v_add_f32 v129, v129, v145\n\t"
    "v_add_f32 v130, v130, v146\n\t"
    "v_add_f32 v131, v131, v147\n\t"
    "v_add_f32 v132, v132, v148\n\t"
    "v_add_f32 v133, v133, v149\n\t"
    "v_add_f32 v134, v134, v150\n\t"
    "v_add_f32 v135, v135, v151\n\t"
    "v_add_f32 v160, v160, v176\n\t"
    "v_add_f32 v161, v161, v177\n\t"
    "v_add_f32 v162, v162, v178\n\t"
    "v_add_f32 v163, v163, v179\n\t"
    "v_add_f32 v164, v164, v180\n\t"
    "v_add_f32 v165, v165, v181\n\t"
    "v_add_f32 v166, v166, v182\n\t"
    "v_add_f32 v167, v167, v183\n\t"
    "v_add_f32 v128, v128, v160\n\t"
    "v_add_f32 v129, v129, v161\n\t"
    "v_add_f32 v130, v130, v162\n\t"
    "v_add_f32 v131, v131, v163\n\t"
    "v_add_f32 v132, v132, v164\n\t"
    "v_add_f32 v133, v133, v165\n\t"
    "v_add_f32 v134, v134, v166\n\t"
    "v_add_f32 v135, v135, v167\n\t"
    "v_mul_f32 v128, v197, v128\n\t"
    "v_mul_f32 v129, v197, v129\n\t"
    "v_mul_f32 v130, v197, v130\n\t"
    "v_mul_f32 v131, v197, v131\n\t"
    "v_mul_f32 v132, v197, v132\n\t"
    "v_mul_f32 v133, v197, v133\n\t"
    "v_mul_f32 v134, v197, v134\n\t"
    "v_mul_f32 v135, v197, v135\n\t"
    "v_cvt_pk_bf16_f32 v208, v128, v129\n\t"
    "v_cvt_pk_bf16_f32 v209, v130, v131\n\t"
    "v_cvt_pk_bf16_f32 v210, v132, v133\n\t"
    "v_cvt_pk_bf16_f32 v211, v134, v135\n\t"
    "buffer_store_dwordx4 v[208:211], v21, s[64:67], 0 offen\n\t"
    "s_branch L_end_%=\n\t"
    "L_half0_%=:\n\t"
    "s_waitcnt lgkmcnt(0)\n\t"
    "s_mov_b64 s[36:37], s[12:13]\n\t"
    "s_mov_b32 s38, 0x200000\n\t"
    "s_mov_b32 s39, 0x20000\n\t"
    "s_mov_b64 s[40:41], s[14:15]\n\t"
    "s_mov_b32 s42, 0x180000\n\t"
    "s_mov_b32 s43, 0x20000\n\t"
    "s_add_u32 s11, s74, 1\n\t"
    "s_and_b32 s75, s11, 7\n\t"
    "s_add_u32 s11, s74, 2\n\t"
    "s_and_b32 s76, s11, 7\n\t"
    "s_add_u32 s11, s74, 3\n\t"
    "s_and_b32 s77, s11, 7\n\t"
    "s_add_u32 s11, s74, 4\n\t"
    "s_and_b32 s78, s11, 7\n\t"
    "s_add_u32 s11, s74, 5\n\t"
    "s_and_b32 s79, s11, 7\n\t"
    "s_add_u32 s11, s74, 6\n\t"
    "s_and_b32 s80, s11, 7\n\t"
    "s_add_u32 s11, s74, 7\n\t"
    "s_and_b32 s81, s11, 7\n\t"
    "s_mov_b64 s[52:53], s[28:29]\n\t"
    "s_mov_b32 s54, 0x1200000\n\t"
    "s_mov_b32 s55, 0x20000\n\t"
    "s_mov_b64 s[44:45], s[18:19]\n\t"
    "s_mov_b32 s46, 0x1c0000\n\t"
    "s_mov_b32 s47, 0x20000\n\t"
    "s_mov_b64 s[48:49], s[20:21]\n\t"
    "s_mov_b32 s50, 0x1c0000\n\t"
    "s_mov_b32 s51, 0x20000\n\t"
    "s_mov_b64 s[68:69], s[24:25]\n\t"
    "s_mov_b32 s70, 0x400\n\t"
    "s_mov_b32 s71, 0x20000\n\t"
    "s_mov_b64 s[56:57], s[22:23]\n\t"
    "s_mov_b32 s58, 0x1c0000\n\t"
    "s_mov_b32 s59, 0x20000\n\t"
    "s_mov_b64 s[60:61], s[16:17]\n\t"
    "s_mov_b32 s62, 0x80000\n\t"
    "s_mov_b32 s63, 0x20000\n\t"
    "s_mov_b64 s[64:65], s[26:27]\n\t"
    "s_mov_b32 s66, 0x200000\n\t"
    "s_mov_b32 s67, 0x20000\n\t"
    "v_lshlrev_b32 v251, 7, v1\n\t"
    "s_lshl_b32 s11, %1, 2\n\t"
    "s_add_u32 s11, s11, s8\n\t"
    "s_mov_b64 exec, -1\n\t"
    "v_lshlrev_b32 v203, 4, v1\n\t"
    "s_mov_b32 s11, 0x1005000\n\t"
    "s_buffer_load_dwordx2 s[82:83], s[52:55], s11\n\t"
    "s_mov_b32 s84, 0\n\t"
    "s_and_b32 s31, s8, 3\n\t"
    "v_and_b32 v208, 31, v1\n\t"
    "v_lshrrev_b32 v209, 5, v1\n\t"
    "v_bfe_u32 v210, v1, 1, 3\n\t"
    "v_xor_b32 v210, v210, v209\n\t"
    "v_lshlrev_b32 v210, 4, v210\n\t"
    "v_lshl_or_b32 v2, v208, 7, v210\n\t"
    "s_lshl_b32 s11, s31, 12\n\t"
    "v_add_u32 v2, s11, v2\n\t"
    "v_xor_b32 v3, 32, v2\n\t"
    "v_xor_b32 v4, 64, v2\n\t"
    "v_xor_b32 v5, 0x60, v2\n\t"
    "v_add_u32 v6, 0x10000, v2\n\t"
    "v_add_u32 v7, 0x10000, v3\n\t"
    "v_add_u32 v8, 0x10000, v4\n\t"
    "v_add_u32 v9, 0x10000, v5\n\t"
    "v_bfe_u32 v210, v1, 2, 2\n\t"
    "v_lshl_add_u32 v210, v209, 3, v210\n\t"
    "v_bfe_u32 v211, v1, 4, 1\n\t"
    "v_and_b32 v212, 3, v1\n\t"
    "v_lshlrev_b32 v212, 3, v212\n\t"
    "v_lshl_add_u32 v13, v210, 6, v212\n\t"
    "v_lshl_add_u32 v13, v211, 5, v13\n\t"
    "v_add_u32 v13, 0x16000, v13\n\t"
    "v_add_u32 v14, 0xa000, v13\n\t"
    "v_lshl_add_u32 v10, v210, 7, v212\n\t"
    "v_lshl_add_u32 v10, v211, 5, v10\n\t"
    "v_bfe_u32 v213, v1, 3, 1\n\t"
    "v_lshl_add_u32 v10, v213, 6, v10\n\t"
    "v_add_u32 v10, 0x14000, v10\n\t"
    "v_add_u32 v11, 0xa000, v10\n\t"
    "v_xor_b32 v22, 64, v10\n\t"
    "v_xor_b32 v23, 64, v11\n\t"
    "s_and_b32 s12, %1, 7\n\t"
    "s_lshl_b32 s12, s12, 2\n\t"
    "s_bfe_u32 s11, %1, 0x20003\n\t"
    "s_add_u32 s12, s12, s11\n\t"
    "s_bfe_u32 s13, %1, 0x30005\n\t"
    "s_lshr_b32 s14, s10, 3\n\t"
    "s_lshl_b32 s11, s9, 1\n\t"
    "s_add_u32 s14, s14, s11\n\t"
    "s_lshl_b32 s11, s13, 3\n\t"
    "s_add_u32 s11, s11, s8\n\t"
    "s_mul_i32 s11, s11, 0x90\n\t"
    "s_add_u32 s11, s11, s12\n\t"
    "s_lshl_b32 s11, s11, 10\n\t"
    "s_add_u32 s11, s11, 0x61c000\n\t"
    "v_add_u32 v19, s11, v203\n\t"
    "s_and_b32 s31, s8, 3\n\t"
    "v_mul_u32_u24 v210, 0x124a, v1\n\t"
    "v_lshrrev_b32 v210, 16, v210\n\t"
    "v_mul_u32_u24 v211, 14, v210\n\t"
    "v_sub_u32 v211, v1, v211\n\t"
    "v_mul_u32_u24 v210, 0xe00, v210\n\t"
    "v_lshl_add_u32 v15, v211, 4, v210\n\t"
    "s_mul_i32 s11, s8, 0x3800\n\t"
    "s_mul_i32 s30, s10, 0xe0\n\t"
    "s_add_u32 s11, s11, s30\n\t"
    "v_add_u32 v15, s11, v15\n\t"
    "s_and_b32 s35, s8, 1\n\t"
    "s_lshl_b32 s35, s35, 2\n\t"
    "s_lshr_b32 s11, s8, 1\n\t"
    "s_add_u32 s35, s35, s11\n\t"
    "s_mul_i32 s35, s35, 0x380\n\t"
    "s_mov_b32 s72, -1\n\t"
    "s_mov_b32 s73, 0xffffff\n\t"
    "s_lshl_b32 s11, s9, 2\n\t"
    "s_add_u32 s11, s11, s31\n\t"
    "s_mul_i32 s31, s11, 0x24000\n\t"
    "s_lshl_b32 s11, s11, 10\n\t"
    "s_lshl_b32 s30, s10, 17\n\t"
    "s_add_u32 s30, s30, s11\n\t"
    "s_add_u32 s30, s30, 0x400000\n\t"
    "v_and_b32 v208, 15, v1\n\t"
    "v_lshlrev_b32 v208, 4, v208\n\t"
    "v_lshrrev_b32 v209, 5, v1\n\t"
    "v_lshl_or_b32 v208, v209, 8, v208\n\t"
    "v_bfe_u32 v209, v1, 4, 1\n\t"
    "v_lshl_or_b32 v208, v209, 16, v208\n\t"
    "v_add_u32 v17, s30, v208\n\t"
    "s_lshr_b32 s30, s10, 1\n\t"
    "s_lshl_b32 s30, s30, 18\n\t"
    "s_add_u32 s11, s11, s30\n\t"
    "v_and_b32 v208, 51, v1\n\t"
    "v_bfe_u32 v209, v1, 2, 1\n\t"
    "v_lshl_or_b32 v208, v209, 3, v208\n\t"
    "v_bfe_u32 v209, v1, 3, 1\n\t"
    "v_lshl_or_b32 v208, v209, 2, v208\n\t"
    "s_bitcmp1_b32 s10, 0\n\t"
    "s_cselect_b64 vcc, -1, 0\n\t"
    "s_cselect_b32 s30, 0x200000, 0\n\t"
    "s_cselect_b32 s85, 1.0, 0x3eb8aa3b\n\t"
    "s_add_u32 s11, s11, s30\n\t"
    "v_cndmask_b32 v208, v1, v208, vcc\n\t"
    "v_lshlrev_b32 v16, 4, v208\n\t"
    "v_add_u32 v16, s11, v16\n\t"
    "s_mul_i32 s30, s10, 0x1c00\n\t"
    "s_add_u32 s31, s31, s30\n\t"
    "s_add_u32 s31, s31, 0x600000\n\t"
    "v_add_u32 v18, s31, v203\n\t"
    "s_lshl_b32 s16, s8, 11\n\t"
    "s_lshl_b32 s11, s12, 16\n\t"
    "s_add_u32 s11, s11, s16\n\t"
    "v_add_u32 v199, s11, v203\n\t"
    "v_mov_b32 v208, -1\n\t"
    "v_mov_b32 v209, -1\n\t"
    "v_mov_b32 v210, -1\n\t"
    "v_mov_b32 v211, -1\n\t"
    "buffer_store_dwordx4 v[208:211], v19, s[52:55], 0 offen sc1\n\t"
    "s_waitcnt lgkmcnt(0)\n\t"
    "s_add_u32 s82, s82, 1\n\t"
    "s_barrier\n\t"
    "ds_read_b128 v[24:27], v2\n\t"
    "ds_read_b64_tr_b16 v[32:33], v11 offset:4096\n\t"
    "ds_read_b64_tr_b16 v[34:35], v11 offset:4608\n\t"
    "ds_read_b64_tr_b16 v[36:37], v23 offset:4096\n\t"
    "ds_read_b64_tr_b16 v[38:39], v23 offset:4608\n\t"
    "ds_read_b64_tr_b16 v[40:41], v14 offset:4096\n\t"
    "ds_read_b64_tr_b16 v[42:43], v14 offset:4352\n\t"
    "ds_read_b128 v[28:31], v3\n\t"
    "ds_read_b64_tr_b16 v[48:49], v11 offset:6144\n\t"
    "ds_read_b64_tr_b16 v[50:51], v11 offset:6656\n\t"
    "ds_read_b64_tr_b16 v[52:53], v23 offset:6144\n\t"
    "ds_read_b64_tr_b16 v[54:55], v23 offset:6656\n\t"
    "ds_read_b64_tr_b16 v[56:57], v14 offset:5120\n\t"
    "ds_read_b64_tr_b16 v[58:59], v14 offset:5376\n\t"
    "s_waitcnt lgkmcnt(11)\n\t"
    "v_mfma_f32_32x32x16_bf16 v[128:143], v[32:35], v[24:27], 0\n\t"
    "s_mov_b32 s18, 0xffff\n\t"
    "s_mov_b32 s19, 0xffff\n\t"
    "s_waitcnt lgkmcnt(9)\n\t"
    "v_mfma_f32_32x32x16_bf16 v[144:159], v[36:39], v[24:27], 0\n\t"
    "v_add_u32 v204, 0x4000, v203\n\t"
    "v_and_b32 v248, 15, v1\n\t"
    "s_waitcnt lgkmcnt(7)\n\t"
    "v_mfma_f32_32x32x16_bf16 v[160:175], v[24:27], v[40:43], 0\n\t"
    "v_lshlrev_b32 v248, 4, v248\n\t"
    "v_lshrrev_b32 v249, 5, v1\n\t"
    "ds_read_b128 v[24:27], v4\n\t"
    "ds_read_b64_tr_b16 v[32:33], v11 offset:8192\n\t"
    "ds_read_b64_tr_b16 v[34:35], v11 offset:8704\n\t"
    "ds_read_b64_tr_b16 v[36:37], v23 offset:8192\n\t"
    "ds_read_b64_tr_b16 v[38:39], v23 offset:8704\n\t"
    "ds_read_b64_tr_b16 v[40:41], v14 offset:6144\n\t"
    "ds_read_b64_tr_b16 v[42:43], v14 offset:6400\n\t"
    "s_waitcnt lgkmcnt(11)\n\t"
    "v_mfma_f32_32x32x16_bf16 v[128:143], v[48:51], v[28:31], v[128:143]\n\t"
    "v_lshl_or_b32 v205, v249, 8, v248\n\t"
    "v_add_u32 v205, 0x8000, v205\n\t"
    "s_waitcnt lgkmcnt(9)\n\t"
    "v_mfma_f32_32x32x16_bf16 v[144:159], v[52:55], v[28:31], v[144:159]\n\t"
    "v_and_b32 v248, 27, v1\n\t"
    "v_cmp_eq_u32 vcc, 16, v248\n\t"
    "s_waitcnt lgkmcnt(7)\n\t"
    "v_mfma_f32_32x32x16_bf16 v[160:175], v[28:31], v[56:59], v[160:175]\n\t"
    "v_mov_b32 v250, 0x3f803f80\n\t"
    "v_mov_b32 v206, 0xdddddddd\n\t"
    "ds_read_b128 v[28:31], v5\n\t"
    "ds_read_b64_tr_b16 v[48:49], v11 offset:10240\n\t"
    "ds_read_b64_tr_b16 v[50:51], v11 offset:10752\n\t"
    "ds_read_b64_tr_b16 v[52:53], v23 offset:10240\n\t"
    "ds_read_b64_tr_b16 v[54:55], v23 offset:10752\n\t"
    "ds_read_b64_tr_b16 v[56:57], v14 offset:7168\n\t"
    "ds_read_b64_tr_b16 v[58:59], v14 offset:7424\n\t"
    "s_waitcnt lgkmcnt(11)\n\t"
    "v_mfma_f32_32x32x16_bf16 v[128:143], v[32:35], v[24:27], v[128:143]\n\t"
    "v_cndmask_b32 v180, 0, v250, vcc\n\t"
    "v_mov_b32 v181, v180\n\t"
    "s_waitcnt lgkmcnt(9)\n\t"
    "v_mfma_f32_32x32x16_bf16 v[144:159], v[36:39], v[24:27], v[144:159]\n\t"
    "v_mov_b32 v182, v180\n\t"
    "v_mov_b32 v183, v180\n\t"
    "s_waitcnt lgkmcnt(7)\n\t"
    "v_mfma_f32_32x32x16_bf16 v[160:175], v[24:27], v[40:43], v[160:175]\n\t"
    "v_mov_b32 v184, v180\n\t"
    "v_mov_b32 v185, v180\n\t"
    "s_waitcnt lgkmcnt(0)\n\t"
    "s_barrier\n\t"
    "ds_read_b128 v[24:27], v2 offset:16384\n\t"
    "ds_read_b64_tr_b16 v[32:33], v11 offset:16384\n\t"
    "ds_read_b64_tr_b16 v[34:35], v11 offset:16896\n\t"
    "ds_read_b64_tr_b16 v[36:37], v23 offset:16384\n\t"
    "ds_read_b64_tr_b16 v[38:39], v23 offset:16896\n\t"
    "ds_read_b64_tr_b16 v[40:41], v14 offset:16384\n\t"
    "ds_read_b64_tr_b16 v[42:43], v14 offset:16640\n\t"
    "v_mfma_f32_32x32x16_bf16 v[128:143], v[48:51], v[28:31], v[128:143]\n\t"
    "v_mov_b32 v186, v180\n\t"
    "v_mov_b32 v187, v180\n\t"
    "v_mfma_f32_32x32x16_bf16 v[144:159], v[52:55], v[28:31], v[144:159]\n\t"
    "v_mov_b32 v188, v180\n\t"
    "v_mov_b32 v189, v180\n\t"
    "v_mfma_f32_32x32x16_bf16 v[160:175], v[28:31], v[56:59], v[160:175]\n\t"
    "v_mov_b32 v190, v180\n\t"
    "v_mov_b32 v191, v180\n\t"
    "ds_read_b128 v[28:31], v3 offset:16384\n\t"
    "ds_read_b64_tr_b16 v[48:49], v11 offset:18432\n\t"
    "ds_read_b64_tr_b16 v[50:51], v11 offset:18944\n\t"
    "ds_read_b64_tr_b16 v[52:53], v23 offset:18432\n\t"
    "ds_read_b64_tr_b16 v[54:55], v23 offset:18944\n\t"
    "ds_read_b64_tr_b16 v[56:57], v14 offset:17408\n\t"
    "ds_read_b64_tr_b16 v[58:59], v14 offset:17664\n\t"
    "s_waitcnt lgkmcnt(11)\n\t"
    "v_mfma_f32_32x32x16_bf16 v[128:143], v[32:35], v[24:27], v[128:143]\n\t"
    "v_bfe_u32 v248, v1, 4, 1\n\t"
    "v_lshlrev_b32 v248, 2, v248\n\t"
    "s_waitcnt lgkmcnt(9)\n\t"
    "v_mfma_f32_32x32x16_bf16 v[144:159], v[36:39], v[24:27], v[144:159]\n\t"
    "v_xor_b32 v248, v248, v1\n\t"
    "v_and_b32 v248, 7, v248\n\t"
    "s_waitcnt lgkmcnt(7)\n\t"
    "v_mfma_f32_32x32x16_bf16 v[160:175], v[24:27], v[40:43], v[160:175]\n\t"
    "v_lshrrev_b32 v250, 3, v1\n\t"
    "v_lshlrev_b32 v250, 10, v250\n\t"
    "ds_read_b128 v[24:27], v4 offset:16384\n\t"
    "ds_read_b64_tr_b16 v[32:33], v11 offset:20480\n\t"
    "ds_read_b64_tr_b16 v[34:35], v11 offset:20992\n\t"
    "ds_read_b64_tr_b16 v[36:37], v23 offset:20480\n\t"
    "ds_read_b64_tr_b16 v[38:39], v23 offset:20992\n\t"
    "ds_read_b64_tr_b16 v[40:41], v14 offset:18432\n\t"
    "ds_read_b64_tr_b16 v[42:43], v14 offset:18688\n\t"
    "s_waitcnt lgkmcnt(11)\n\t"
    "v_mfma_f32_32x32x16_bf16 v[128:143], v[48:51], v[28:31], v[128:143]\n\t"
    "v_lshl_or_b32 v207, v248, 4, v250\n\t"
    "s_and_b32 s17, s10, 7\n\t"
    "s_waitcnt lgkmcnt(9)\n\t"
    "v_mfma_f32_32x32x16_bf16 v[144:159], v[52:55], v[28:31], v[144:159]\n\t"
    "s_lshl_b32 s17, s17, 7\n\t"
    "s_lshl_b32 s26, s8, 14\n\t"
    "s_waitcnt lgkmcnt(7)\n\t"
    "v_mfma_f32_32x32x16_bf16 v[160:175], v[28:31], v[56:59], v[160:175]\n\t"
    "s_add_u32 s17, s17, s26\n\t"
    "v_add_u32 v207, s17, v207\n\t"
    "ds_read_b128 v[28:31], v5 offset:16384\n\t"
    "ds_read_b64_tr_b16 v[48:49], v11 offset:22528\n\t"
    "ds_read_b64_tr_b16 v[50:51], v11 offset:23040\n\t"
    "ds_read_b64_tr_b16 v[52:53], v23 offset:22528\n\t"
    "ds_read_b64_tr_b16 v[54:55], v23 offset:23040\n\t"
    "ds_read_b64_tr_b16 v[56:57], v14 offset:19456\n\t"
    "ds_read_b64_tr_b16 v[58:59], v14 offset:19712\n\t"
    "s_waitcnt lgkmcnt(11)\n\t"
    "v_mfma_f32_32x32x16_bf16 v[128:143], v[32:35], v[24:27], v[128:143]\n\t"
    "s_and_b32 s15, s8, 3\n\t"
    "s_lshl_b32 s15, s15, 12\n\t"
    "s_waitcnt lgkmcnt(9)\n\t"
    "v_mfma_f32_32x32x16_bf16 v[144:159], v[36:39], v[24:27], v[144:159]\n\t"
    "s_lshr_b32 s17, s8, 2\n\t"
    "s_mul_i32 s17, s17, 0x24000\n\t"
    "s_waitcnt lgkmcnt(7)\n\t"
    "v_mfma_f32_32x32x16_bf16 v[160:175], v[24:27], v[40:43], v[160:175]\n\t"
    "s_add_u32 s15, s15, s17\n\t"
    "s_lshl_b32 s27, s8, 13\n\t"
    "s_waitcnt lgkmcnt(0)\n\t"
    "s_barrier\n\t"
    "ds_read_b128 v[24:27], v2 offset:32768\n\t"
    "ds_read_b64_tr_b16 v[32:33], v11 offset:28672\n\t"
    "ds_read_b64_tr_b16 v[34:35], v11 offset:29184\n\t"
    "ds_read_b64_tr_b16 v[36:37], v23 offset:28672\n\t"
    "ds_read_b64_tr_b16 v[38:39], v23 offset:29184\n\t"
    "ds_read_b64_tr_b16 v[40:41], v14 offset:28672\n\t"
    "ds_read_b64_tr_b16 v[42:43], v14 offset:28928\n\t"
    "v_mfma_f32_32x32x16_bf16 v[128:143], v[48:51], v[28:31], v[128:143]\n\t"
    "s_add_u32 s27, s27, 0x4000\n\t"
    "s_lshl_b32 s29, s8, 12\n\t"
    "v_mfma_f32_32x32x16_bf16 v[144:159], v[52:55], v[28:31], v[144:159]\n\t"
    "s_add_u32 s29, s29, 0x14000\n\t"
    "v_bfe_u32 v248, v1, 2, 2\n\t"
    "v_mfma_f32_32x32x16_bf16 v[160:175], v[28:31], v[56:59], v[160:175]\n\t"
    "v_lshl_add_u32 v248, v249, 3, v248\n\t"
    "v_lshlrev_b32 v252, 7, v248\n\t"
    "ds_read_b128 v[28:31], v3 offset:32768\n\t"
    "ds_read_b64_tr_b16 v[48:49], v11 offset:30720\n\t"
    "ds_read_b64_tr_b16 v[50:51], v11 offset:31232\n\t"
    "ds_read_b64_tr_b16 v[52:53], v23 offset:30720\n\t"
    "ds_read_b64_tr_b16 v[54:55], v23 offset:31232\n\t"
    "ds_read_b64_tr_b16 v[56:57], v14 offset:29696\n\t"
    "ds_read_b64_tr_b16 v[58:59], v14 offset:29952\n\t"
    "s_waitcnt lgkmcnt(11)\n\t"
    "v_mfma_f32_32x32x16_bf16 v[128:143], v[32:35], v[24:27], v[128:143]\n\t"
    "v_bfe_u32 v248, v1, 3, 1\n\t"
    "v_lshl_or_b32 v252, v248, 6, v252\n\t"
    "s_waitcnt lgkmcnt(9)\n\t"
    "v_mfma_f32_32x32x16_bf16 v[144:159], v[36:39], v[24:27], v[144:159]\n\t"
    "v_bfe_u32 v248, v1, 4, 1\n\t"
    "v_lshl_or_b32 v252, v248, 5, v252\n\t"
    "s_waitcnt lgkmcnt(7)\n\t"
    "v_mfma_f32_32x32x16_bf16 v[160:175], v[24:27], v[40:43], v[160:175]\n\t"
    "v_and_b32 v248, 1, v1\n\t"
    "v_lshl_or_b32 v252, v248, 4, v252\n\t"
    "ds_read_b128 v[24:27], v4 offset:32768\n\t"
    "ds_read_b64_tr_b16 v[32:33], v11 offset:32768\n\t"
    "ds_read_b64_tr_b16 v[34:35], v11 offset:33280\n\t"
    "ds_read_b64_tr_b16 v[36:37], v23 offset:32768\n\t"
    "ds_read_b64_tr_b16 v[38:39], v23 offset:33280\n\t"
    "ds_read_b64_tr_b16 v[40:41], v14 offset:30720\n\t"
    "ds_read_b64_tr_b16 v[42:43], v14 offset:30976\n\t"
    "s_waitcnt lgkmcnt(11)\n\t"
    "v_mfma_f32_32x32x16_bf16 v[128:143], v[48:51], v[28:31], v[128:143]\n\t"
    "v_bfe_u32 v248, v1, 1, 1\n\t"
    "v_lshl_or_b32 v252, v248, 3, v252\n\t"
    "s_waitcnt lgkmcnt(9)\n\t"
    "v_mfma_f32_32x32x16_bf16 v[144:159], v[52:55], v[28:31], v[144:159]\n\t"
    "v_add_u32 v252, s15, v252\n\t"
    "v_xor_b32 v253, 64, v252\n\t"
    "s_waitcnt lgkmcnt(7)\n\t"
    "v_mfma_f32_32x32x16_bf16 v[160:175], v[28:31], v[56:59], v[160:175]\n\t"
    "s_mul_i32 s17, s14, 0x120\n\t"
    "s_add_u32 s17, s17, s8\n\t"
    "ds_read_b128 v[28:31], v5 offset:32768\n\t"
    "ds_read_b64_tr_b16 v[48:49], v11 offset:34816\n\t"
    "ds_read_b64_tr_b16 v[50:51], v11 offset:35328\n\t"
    "ds_read_b64_tr_b16 v[52:53], v23 offset:34816\n\t"
    "ds_read_b64_tr_b16 v[54:55], v23 offset:35328\n\t"
    "ds_read_b64_tr_b16 v[56:57], v14 offset:31744\n\t"
    "ds_read_b64_tr_b16 v[58:59], v14 offset:32000\n\t"
    "s_waitcnt lgkmcnt(11)\n\t"
    "v_mfma_f32_32x32x16_bf16 v[128:143], v[32:35], v[24:27], v[128:143]\n\t"
    "s_lshl_b32 s17, s17, 10\n\t"
    "s_add_u32 s17, s17, 0x600000\n\t"
    "s_waitcnt lgkmcnt(9)\n\t"
    "v_mfma_f32_32x32x16_bf16 v[144:159], v[36:39], v[24:27], v[144:159]\n\t"
    "v_add_u32 v20, s17, v203\n\t"
    "s_lshl_b32 s5, s14, 1\n\t"
    "s_waitcnt lgkmcnt(7)\n\t"
    "v_mfma_f32_32x32x16_bf16 v[160:175], v[24:27], v[40:43], v[160:175]\n\t"
    "s_lshr_b32 s6, s8, 2\n\t"
    "s_add_u32 s5, s5, s6\n\t"
    "s_waitcnt lgkmcnt(0)\n\t"
    "s_barrier\n\t"
    "ds_read_b128 v[24:27], v2 offset:49152\n\t"
    "ds_read_b64_tr_b16 v[32:33], v10 offset:20480\n\t"
    "ds_read_b64_tr_b16 v[34:35], v10 offset:20992\n\t"
    "ds_read_b64_tr_b16 v[36:37], v22 offset:20480\n\t"
    "ds_read_b64_tr_b16 v[38:39], v22 offset:20992\n\t"
    "ds_read_b64_tr_b16 v[40:41], v13 offset:20480\n\t"
    "ds_read_b64_tr_b16 v[42:43], v13 offset:20736\n\t"
    "v_mfma_f32_32x32x16_bf16 v[128:143], v[48:51], v[28:31], v[128:143]\n\t"
    "s_lshl_b32 s5, s5, 15\n\t"
    "s_and_b32 s6, s10, 7\n\t"
    "v_mfma_f32_32x32x16_bf16 v[144:159], v[52:55], v[28:31], v[144:159]\n\t"
    "s_lshl_b32 s6, s6, 7\n\t"
    "s_add_u32 s5, s5, s6\n\t"
    "v_mfma_f32_32x32x16_bf16 v[160:175], v[28:31], v[56:59], v[160:175]\n\t"
    "s_and_b32 s6, s8, 3\n\t"
    "s_lshl_b32 s6, s6, 5\n\t"
    "ds_read_b128 v[28:31], v3 offset:49152\n\t"
    "ds_read_b64_tr_b16 v[48:49], v10 offset:22528\n\t"
    "ds_read_b64_tr_b16 v[50:51], v10 offset:23040\n\t"
    "ds_read_b64_tr_b16 v[52:53], v22 offset:22528\n\t"
    "ds_read_b64_tr_b16 v[54:55], v22 offset:23040\n\t"
    "ds_read_b64_tr_b16 v[56:57], v13 offset:21504\n\t"
    "ds_read_b64_tr_b16 v[58:59], v13 offset:21760\n\t"
    "s_waitcnt lgkmcnt(11)\n\t"
    "v_mfma_f32_32x32x16_bf16 v[128:143], v[32:35], v[24:27], v[128:143]\n\t"
    "s_add_u32 s5, s5, s6\n\t"
    "v_and_b32 v248, 31, v1\n\t"
    "s_waitcnt lgkmcnt(9)\n\t"
    "v_mfma_f32_32x32x16_bf16 v[144:159], v[36:39], v[24:27], v[144:159]\n\t"
    "v_lshlrev_b32 v248, 10, v248\n\t"
    "v_lshrrev_b32 v249, 5, v1\n\t"
    "s_waitcnt lgkmcnt(7)\n\t"
    "v_mfma_f32_32x32x16_bf16 v[160:175], v[24:27], v[40:43], v[160:175]\n\t"
    "v_lshl_or_b32 v21, v249, 4, v248\n\t"
    "v_add_u32 v21, s5, v21\n\t"
    "ds_read_b128 v[24:27], v4 offset:49152\n\t"
    "ds_read_b64_tr_b16 v[32:33], v10 offset:24576\n\t"
    "ds_read_b64_tr_b16 v[34:35], v10 offset:25088\n\t"
    "ds_read_b64_tr_b16 v[36:37], v22 offset:24576\n\t"
    "ds_read_b64_tr_b16 v[38:39], v22 offset:25088\n\t"
    "ds_read_b64_tr_b16 v[40:41], v13 offset:22528\n\t"
    "ds_read_b64_tr_b16 v[42:43], v13 offset:22784\n\t"
    "s_waitcnt lgkmcnt(11)\n\t"
    "v_mfma_f32_32x32x16_bf16 v[128:143], v[48:51], v[28:31], v[128:143]\n\t"
    "v_lshrrev_b32 v208, 2, v1\n\t"
    "v_mul_u32_u24 v208, 0xe0, v208\n\t"
    "s_waitcnt lgkmcnt(9)\n\t"
    "v_mfma_f32_32x32x16_bf16 v[144:159], v[52:55], v[28:31], v[144:159]\n\t"
    "v_and_b32 v209, 3, v1\n\t"
    "v_lshl_add_u32 v12, v209, 3, v208\n\t"
    "s_waitcnt lgkmcnt(7)\n\t"
    "v_mfma_f32_32x32x16_bf16 v[160:175], v[28:31], v[56:59], v[160:175]\n\t"
    "v_add_u32 v12, 0x14000, v12\n\t"
    "v_add_u32 v255, 0xab00, v12\n\t"
    "ds_read_b128 v[28:31], v5 offset:49152\n\t"
    "ds_read_b64_tr_b16 v[48:49], v10 offset:26624\n\t"
    "ds_read_b64_tr_b16 v[50:51], v10 offset:27136\n\t"
    "ds_read_b64_tr_b16 v[52:53], v22 offset:26624\n\t"
    "ds_read_b64_tr_b16 v[54:55], v22 offset:27136\n\t"
    "ds_read_b64_tr_b16 v[56:57], v13 offset:23552\n\t"
    "ds_read_b64_tr_b16 v[58:59], v13 offset:23808\n\t"
    "s_waitcnt lgkmcnt(11)\n\t"
    "v_mfma_f32_32x32x16_bf16 v[128:143], v[32:35], v[24:27], v[128:143]\n\t"
    "v_bfe_u32 v208, v1, 1, 3\n\t"
    "v_lshrrev_b32 v209, 4, v1\n\t"
    "s_waitcnt lgkmcnt(9)\n\t"
    "v_mfma_f32_32x32x16_bf16 v[144:159], v[36:39], v[24:27], v[144:159]\n\t"
    "v_xor_b32 v208, v208, v209\n\t"
    "v_lshlrev_b32 v208, 4, v208\n\t"
    "s_waitcnt lgkmcnt(7)\n\t"
    "v_mfma_f32_32x32x16_bf16 v[160:175], v[24:27], v[40:43], v[160:175]\n\t"
    "v_and_b32 v209, 15, v1\n\t"
    "v_lshl_or_b32 v248, v209, 7, v208\n\t"
    "s_waitcnt lgkmcnt(0)\n\t"
    "s_barrier\n\t"
    "ds_read_b128 v[24:27], v6\n\t"
    "ds_read_b64_tr_b16 v[32:33], v10 offset:32768\n\t"
    "ds_read_b64_tr_b16 v[34:35], v10 offset:33280\n\t"
    "ds_read_b64_tr_b16 v[36:37], v22 offset:32768\n\t"
    "ds_read_b64_tr_b16 v[38:39], v22 offset:33280\n\t"
    "ds_read_b64_tr_b16 v[40:41], v13 offset:32768\n\t"
    "ds_read_b64_tr_b16 v[42:43], v13 offset:33024\n\t"
    "v_mfma_f32_32x32x16_bf16 v[128:143], v[48:51], v[28:31], v[128:143]\n\t"
    "s_and_b32 s5, s8, 3\n\t"
    "s_lshl_b32 s5, s5, 12\n\t"
    "v_mfma_f32_32x32x16_bf16 v[144:159], v[52:55], v[28:31], v[144:159]\n\t"
    "v_add_u32 v248, s5, v248\n\t"
    "v_xor_b32 v249, 64, v248\n\t"
    "v_mfma_f32_32x32x16_bf16 v[160:175], v[28:31], v[56:59], v[160:175]\n\t"
    "v_add_u32 v250, 0x10000, v248\n\t"
    "v_add_u32 v251, 0x10000, v249\n\t"
    "ds_read_b128 v[28:31], v7\n\t"
    "ds_read_b64_tr_b16 v[48:49], v10 offset:34816\n\t"
    "ds_read_b64_tr_b16 v[50:51], v10 offset:35328\n\t"
    "ds_read_b64_tr_b16 v[52:53], v22 offset:34816\n\t"
    "ds_read_b64_tr_b16 v[54:55], v22 offset:35328\n\t"
    "ds_read_b64_tr_b16 v[56:57], v13 offset:33792\n\t"
    "ds_read_b64_tr_b16 v[58:59], v13 offset:34048\n\t"
    "s_waitcnt lgkmcnt(11)\n\t"
    "v_mfma_f32_32x32x16_bf16 v[128:143], v[32:35], v[24:27], v[128:143]\n\t"
    "s_waitcnt lgkmcnt(9)\n\t"
    "v_mfma_f32_32x32x16_bf16 v[144:159], v[36:39], v[24:27], v[144:159]\n\t"
    "s_waitcnt lgkmcnt(7)\n\t"
    "v_mfma_f32_32x32x16_bf16 v[160:175], v[24:27], v[40:43], v[160:175]\n\t"
    "ds_read_b128 v[24:27], v8\n\t"
    "ds_read_b64_tr_b16 v[32:33], v10 offset:36864\n\t"
    "ds_read_b64_tr_b16 v[34:35], v10 offset:37376\n\t"
    "ds_read_b64_tr_b16 v[36:37], v22 offset:36864\n\t"
    "ds_read_b64_tr_b16 v[38:39], v22 offset:37376\n\t"
    "ds_read_b64_tr_b16 v[40:41], v13 offset:34816\n\t"
    "ds_read_b64_tr_b16 v[42:43], v13 offset:35072\n\t"
    "s_waitcnt lgkmcnt(11)\n\t"
    "v_mfma_f32_32x32x16_bf16 v[128:143], v[48:51], v[28:31], v[128:143]\n\t"
    "s_waitcnt lgkmcnt(9)\n\t"
    "v_mfma_f32_32x32x16_bf16 v[144:159], v[52:55], v[28:31], v[144:159]\n\t"
    "s_waitcnt lgkmcnt(7)\n\t"
    "v_mfma_f32_32x32x16_bf16 v[160:175], v[28:31], v[56:59], v[160:175]\n\t"
    "ds_read_b128 v[28:31], v9\n\t"
    "ds_read_b64_tr_b16 v[48:49], v10 offset:38912\n\t"
    "ds_read_b64_tr_b16 v[50:51], v10 offset:39424\n\t"
    "ds_read_b64_tr_b16 v[52:53], v22 offset:38912\n\t"
    "ds_read_b64_tr_b16 v[54:55], v22 offset:39424\n\t"
    "ds_read_b64_tr_b16 v[56:57], v13 offset:35840\n\t"
    "ds_read_b64_tr_b16 v[58:59], v13 offset:36096\n\t"
    "s_waitcnt lgkmcnt(11)\n\t"
    "v_mfma_f32_32x32x16_bf16 v[128:143], v[32:35], v[24:27], v[128:143]\n\t"
    "s_waitcnt lgkmcnt(9)\n\t"
    "v_mfma_f32_32x32x16_bf16 v[144:159], v[36:39], v[24:27], v[144:159]\n\t"
    "s_waitcnt lgkmcnt(7)\n\t"
    "v_mfma_f32_32x32x16_bf16 v[160:175], v[24:27], v[40:43], v[160:175]\n\t"
    "s_waitcnt lgkmcnt(0)\n\t"
    "s_barrier\n\t"
    "ds_read_b128 v[24:27], v2\n\t"
    "ds_read_b64_tr_b16 v[32:33], v11 offset:4096\n\t"
    "ds_read_b64_tr_b16 v[34:35], v11 offset:4608\n\t"
    "ds_read_b64_tr_b16 v[36:37], v23 offset:4096\n\t"
    "ds_read_b64_tr_b16 v[38:39], v23 offset:4608\n\t"
    "ds_read_b64_tr_b16 v[40:41], v14 offset:4096\n\t"
    "ds_read_b64_tr_b16 v[42:43], v14 offset:4352\n\t"
    "v_mfma_f32_32x32x16_bf16 v[128:143], v[48:51], v[28:31], v[128:143]\n\t"
    "v_mfma_f32_32x32x16_bf16 v[144:159], v[52:55], v[28:31], v[144:159]\n\t"
    "v_mfma_f32_32x32x16_bf16 v[160:175], v[28:31], v[56:59], v[160:175]\n\t"
    "ds_read_b128 v[28:31], v3\n\t"
    "ds_read_b64_tr_b16 v[48:49], v11 offset:6144\n\t"
    "ds_read_b64_tr_b16 v[50:51], v11 offset:6656\n\t"
    "ds_read_b64_tr_b16 v[52:53], v23 offset:6144\n\t"
    "ds_read_b64_tr_b16 v[54:55], v23 offset:6656\n\t"
    "ds_read_b64_tr_b16 v[56:57], v14 offset:5120\n\t"
    "ds_read_b64_tr_b16 v[58:59], v14 offset:5376\n\t"
    "s_waitcnt lgkmcnt(11)\n\t"
    "v_mfma_f32_32x32x16_bf16 v[128:143], v[32:35], v[24:27], v[128:143]\n\t"
    "s_waitcnt lgkmcnt(9)\n\t"
    "v_mfma_f32_32x32x16_bf16 v[144:159], v[36:39], v[24:27], v[144:159]\n\t"
    "s_waitcnt lgkmcnt(7)\n\t"
    "v_mfma_f32_32x32x16_bf16 v[160:175], v[24:27], v[40:43], v[160:175]\n\t"
    "ds_read_b128 v[24:27], v4\n\t"
    "ds_read_b64_tr_b16 v[32:33], v11 offset:8192\n\t"
    "ds_read_b64_tr_b16 v[34:35], v11 offset:8704\n\t"
    "ds_read_b64_tr_b16 v[36:37], v23 offset:8192\n\t"
    "ds_read_b64_tr_b16 v[38:39], v23 offset:8704\n\t"
    "ds_read_b64_tr_b16 v[40:41], v14 offset:6144\n\t"
    "ds_read_b64_tr_b16 v[42:43], v14 offset:6400\n\t"
    "s_waitcnt lgkmcnt(11)\n\t"
    "v_mfma_f32_32x32x16_bf16 v[128:143], v[48:51], v[28:31], v[128:143]\n\t"
    "s_waitcnt lgkmcnt(9)\n\t"
    "v_mfma_f32_32x32x16_bf16 v[144:159], v[52:55], v[28:31], v[144:159]\n\t"
    "s_waitcnt lgkmcnt(7)\n\t"
    "v_mfma_f32_32x32x16_bf16 v[160:175], v[28:31], v[56:59], v[160:175]\n\t"
    "ds_read_b128 v[28:31], v5\n\t"
    "ds_read_b64_tr_b16 v[48:49], v11 offset:10240\n\t"
    "ds_read_b64_tr_b16 v[50:51], v11 offset:10752\n\t"
    "ds_read_b64_tr_b16 v[52:53], v23 offset:10240\n\t"
    "ds_read_b64_tr_b16 v[54:55], v23 offset:10752\n\t"
    "ds_read_b64_tr_b16 v[56:57], v14 offset:7168\n\t"
    "ds_read_b64_tr_b16 v[58:59], v14 offset:7424\n\t"
    "s_waitcnt lgkmcnt(11)\n\t"
    "v_mfma_f32_32x32x16_bf16 v[128:143], v[32:35], v[24:27], v[128:143]\n\t"
    "s_waitcnt lgkmcnt(9)\n\t"
    "v_mfma_f32_32x32x16_bf16 v[144:159], v[36:39], v[24:27], v[144:159]\n\t"
    "s_waitcnt lgkmcnt(7)\n\t"
    "v_mfma_f32_32x32x16_bf16 v[160:175], v[24:27], v[40:43], v[160:175]\n\t"
    "s_waitcnt lgkmcnt(0)\n\t"
    "s_barrier\n\t"
    "ds_read_b128 v[24:27], v2 offset:16384\n\t"
    "ds_read_b64_tr_b16 v[32:33], v11 offset:16384\n\t"
    "ds_read_b64_tr_b16 v[34:35], v11 offset:16896\n\t"
    "ds_read_b64_tr_b16 v[36:37], v23 offset:16384\n\t"
    "ds_read_b64_tr_b16 v[38:39], v23 offset:16896\n\t"
    "ds_read_b64_tr_b16 v[40:41], v14 offset:16384\n\t"
    "ds_read_b64_tr_b16 v[42:43], v14 offset:16640\n\t"
    "v_mfma_f32_32x32x16_bf16 v[128:143], v[48:51], v[28:31], v[128:143]\n\t"
    "v_mfma_f32_32x32x16_bf16 v[144:159], v[52:55], v[28:31], v[144:159]\n\t"
    "v_mfma_f32_32x32x16_bf16 v[160:175], v[28:31], v[56:59], v[160:175]\n\t"
    "ds_read_b128 v[28:31], v3 offset:16384\n\t"
    "ds_read_b64_tr_b16 v[48:49], v11 offset:18432\n\t"
    "ds_read_b64_tr_b16 v[50:51], v11 offset:18944\n\t"
    "ds_read_b64_tr_b16 v[52:53], v23 offset:18432\n\t"
    "ds_read_b64_tr_b16 v[54:55], v23 offset:18944\n\t"
    "ds_read_b64_tr_b16 v[56:57], v14 offset:17408\n\t"
    "ds_read_b64_tr_b16 v[58:59], v14 offset:17664\n\t"
    "s_waitcnt lgkmcnt(11)\n\t"
    "v_mfma_f32_32x32x16_bf16 v[128:143], v[32:35], v[24:27], v[128:143]\n\t"
    "s_waitcnt lgkmcnt(9)\n\t"
    "v_mfma_f32_32x32x16_bf16 v[144:159], v[36:39], v[24:27], v[144:159]\n\t"
    "s_waitcnt lgkmcnt(7)\n\t"
    "v_mfma_f32_32x32x16_bf16 v[160:175], v[24:27], v[40:43], v[160:175]\n\t"
    "ds_read_b128 v[24:27], v4 offset:16384\n\t"
    "ds_read_b64_tr_b16 v[32:33], v11 offset:20480\n\t"
    "ds_read_b64_tr_b16 v[34:35], v11 offset:20992\n\t"
    "ds_read_b64_tr_b16 v[36:37], v23 offset:20480\n\t"
    "ds_read_b64_tr_b16 v[38:39], v23 offset:20992\n\t"
    "ds_read_b64_tr_b16 v[40:41], v14 offset:18432\n\t"
    "ds_read_b64_tr_b16 v[42:43], v14 offset:18688\n\t"
    "s_waitcnt lgkmcnt(11)\n\t"
    "v_mfma_f32_32x32x16_bf16 v[128:143], v[48:51], v[28:31], v[128:143]\n\t"
    "s_waitcnt lgkmcnt(9)\n\t"
    "v_mfma_f32_32x32x16_bf16 v[144:159], v[52:55], v[28:31], v[144:159]\n\t"
    "s_waitcnt lgkmcnt(7)\n\t"
    "v_mfma_f32_32x32x16_bf16 v[160:175], v[28:31], v[56:59], v[160:175]\n\t"
    "ds_read_b128 v[28:31], v5 offset:16384\n\t"
    "ds_read_b64_tr_b16 v[48:49], v11 offset:22528\n\t"
    "ds_read_b64_tr_b16 v[50:51], v11 offset:23040\n\t"
    "ds_read_b64_tr_b16 v[52:53], v23 offset:22528\n\t"
    "ds_read_b64_tr_b16 v[54:55], v23 offset:23040\n\t"
    "ds_read_b64_tr_b16 v[56:57], v14 offset:19456\n\t"
    "ds_read_b64_tr_b16 v[58:59], v14 offset:19712\n\t"
    "s_waitcnt lgkmcnt(11)\n\t"
    "v_mfma_f32_32x32x16_bf16 v[128:143], v[32:35], v[24:27], v[128:143]\n\t"
    "s_waitcnt lgkmcnt(9)\n\t"
    "v_mfma_f32_32x32x16_bf16 v[144:159], v[36:39], v[24:27], v[144:159]\n\t"
    "s_waitcnt lgkmcnt(7)\n\t"
    "v_mfma_f32_32x32x16_bf16 v[160:175], v[24:27], v[40:43], v[160:175]\n\t"
    "s_waitcnt lgkmcnt(0)\n\t"
    "s_barrier\n\t"
    "ds_read_b128 v[24:27], v2 offset:32768\n\t"
    "ds_read_b64_tr_b16 v[32:33], v11 offset:28672\n\t"
    "ds_read_b64_tr_b16 v[34:35], v11 offset:29184\n\t"
    "ds_read_b64_tr_b16 v[36:37], v23 offset:28672\n\t"
    "ds_read_b64_tr_b16 v[38:39], v23 offset:29184\n\t"
    "ds_read_b64_tr_b16 v[40:41], v14 offset:28672\n\t"
    "ds_read_b64_tr_b16 v[42:43], v14 offset:28928\n\t"
    "v_mfma_f32_32x32x16_bf16 v[128:143], v[48:51], v[28:31], v[128:143]\n\t"
    "v_mfma_f32_32x32x16_bf16 v[144:159], v[52:55], v[28:31], v[144:159]\n\t"
    "v_mfma_f32_32x32x16_bf16 v[160:175], v[28:31], v[56:59], v[160:175]\n\t"
    "ds_read_b128 v[28:31], v3 offset:32768\n\t"
    "ds_read_b64_tr_b16 v[48:49], v11 offset:30720\n\t"
    "ds_read_b64_tr_b16 v[50:51], v11 offset:31232\n\t"
    "ds_read_b64_tr_b16 v[52:53], v23 offset:30720\n\t"
    "ds_read_b64_tr_b16 v[54:55], v23 offset:31232\n\t"
    "ds_read_b64_tr_b16 v[56:57], v14 offset:29696\n\t"
    "ds_read_b64_tr_b16 v[58:59], v14 offset:29952\n\t"
    "s_waitcnt lgkmcnt(11)\n\t"
    "v_mfma_f32_32x32x16_bf16 v[128:143], v[32:35], v[24:27], v[128:143]\n\t"
    "s_waitcnt lgkmcnt(9)\n\t"
    "v_mfma_f32_32x32x16_bf16 v[144:159], v[36:39], v[24:27], v[144:159]\n\t"
    "s_waitcnt lgkmcnt(7)\n\t"
    "v_mfma_f32_32x32x16_bf16 v[160:175], v[24:27], v[40:43], v[160:175]\n\t"
    "ds_read_b128 v[24:27], v4 offset:32768\n\t"
    "ds_read_b64_tr_b16 v[32:33], v11 offset:32768\n\t"
    "ds_read_b64_tr_b16 v[34:35], v11 offset:33280\n\t"
    "ds_read_b64_tr_b16 v[36:37], v23 offset:32768\n\t"
    "ds_read_b64_tr_b16 v[38:39], v23 offset:33280\n\t"
    "ds_read_b64_tr_b16 v[40:41], v14 offset:30720\n\t"
    "ds_read_b64_tr_b16 v[42:43], v14 offset:30976\n\t"
    "s_waitcnt lgkmcnt(11)\n\t"
    "v_mfma_f32_32x32x16_bf16 v[128:143], v[48:51], v[28:31], v[128:143]\n\t"
    "s_waitcnt lgkmcnt(9)\n\t"
    "v_mfma_f32_32x32x16_bf16 v[144:159], v[52:55], v[28:31], v[144:159]\n\t"
    "s_waitcnt lgkmcnt(7)\n\t"
    "v_mfma_f32_32x32x16_bf16 v[160:175], v[28:31], v[56:59], v[160:175]\n\t"
    "ds_read_b128 v[28:31], v5 offset:32768\n\t"
    "ds_read_b64_tr_b16 v[48:49], v11 offset:34816\n\t"
    "ds_read_b64_tr_b16 v[50:51], v11 offset:35328\n\t"
    "ds_read_b64_tr_b16 v[52:53], v23 offset:34816\n\t"
    "ds_read_b64_tr_b16 v[54:55], v23 offset:35328\n\t"
    "ds_read_b64_tr_b16 v[56:57], v14 offset:31744\n\t"
    "ds_read_b64_tr_b16 v[58:59], v14 offset:32000\n\t"
    "s_waitcnt lgkmcnt(11)\n\t"
    "v_mfma_f32_32x32x16_bf16 v[128:143], v[32:35], v[24:27], v[128:143]\n\t"
    "s_waitcnt lgkmcnt(9)\n\t"
    "v_mfma_f32_32x32x16_bf16 v[144:159], v[36:39], v[24:27], v[144:159]\n\t"
    "s_waitcnt lgkmcnt(7)\n\t"
    "v_mfma_f32_32x32x16_bf16 v[160:175], v[24:27], v[40:43], v[160:175]\n\t"
    "s_waitcnt lgkmcnt(4)\n\t"
    "v_mfma_f32_32x32x16_bf16 v[128:143], v[48:51], v[28:31], v[128:143]\n\t"
    "s_waitcnt lgkmcnt(2)\n\t"
    "v_mfma_f32_32x32x16_bf16 v[144:159], v[52:55], v[28:31], v[144:159]\n\t"
    "s_waitcnt lgkmcnt(0)\n\t"
    "v_mfma_f32_32x32x16_bf16 v[160:175], v[28:31], v[56:59], v[160:175]\n\t"
    "s_barrier\n\t"
    "ds_read_b128 v[24:27], v248 offset:49152\n\t"
    "ds_read_b128 v[28:31], v248 offset:51200\n\t"
    "ds_read_b64_tr_b16 v[32:33], v12\n\t"
    "ds_read_b64_tr_b16 v[34:35], v12 offset:3584\n\t"
    "ds_read_b64_tr_b16 v[36:37], v12 offset:7296\n\t"
    "ds_read_b64_tr_b16 v[38:39], v12 offset:10880\n\t"
    "ds_read_b64_tr_b16 v[40:41], v12 offset:32\n\t"
    "ds_read_b64_tr_b16 v[42:43], v12 offset:3616\n\t"
    "ds_read_b64_tr_b16 v[44:45], v12 offset:7328\n\t"
    "ds_read_b64_tr_b16 v[46:47], v12 offset:10912\n\t"
    "ds_read_b64_tr_b16 v[48:49], v12 offset:64\n\t"
    "ds_read_b64_tr_b16 v[50:51], v12 offset:3648\n\t"
    "ds_read_b64_tr_b16 v[52:53], v12 offset:7360\n\t"
    "ds_read_b64_tr_b16 v[54:55], v12 offset:10944\n\t"
    "ds_read_b64_tr_b16 v[56:57], v12 offset:96\n\t"
    "ds_read_b64_tr_b16 v[58:59], v12 offset:3680\n\t"
    "ds_read_b64_tr_b16 v[60:61], v12 offset:7392\n\t"
    "ds_read_b64_tr_b16 v[62:63], v12 offset:10976\n\t"
    "s_mul_i32 s4, s78, 0x38000\n\t"
    "s_add_u32 s4, s4, 0x1c000\n\t"
    "s_add_u32 m0, s35, 0x1eb00\n\t"
    "s_mov_b64 exec, s[72:73]\n\t"
    "buffer_load_dwordx4 v15, s[44:47], s4 offen lds\n\t"
    "s_mov_b64 exec, -1\n\t"
    "s_mul_i32 s4, s78, 0x38000\n\t"
    "s_add_u32 s4, s4, 0x1c000\n\t"
    "s_add_u32 m0, s35, 0x20780\n\t"
    "s_mov_b64 exec, s[72:73]\n\t"
    "buffer_load_dwordx4 v15, s[48:51], s4 offen lds\n\t"
    "s_mov_b64 exec, -1\n\t"
    "s_waitcnt lgkmcnt(14)\n\t"
    "v_mfma_f32_16x16x32_bf16 v[64:67], v[32:35], v[24:27], 0\n\t"
    "ds_read_b128 v[216:219], v249 offset:49152\n\t"
    "ds_read_b128 v[220:223], v249 offset:51200\n\t"
    "v_mul_f32 v128, s85, v128\n\t"
    "v_mul_f32 v129, s85, v129\n\t"
    "v_mfma_f32_16x16x32_bf16 v[68:71], v[32:35], v[28:31], 0\n\t"
    "ds_read_b64_tr_b16 v[32:33], v12 offset:14592\n\t"
    "ds_read_b64_tr_b16 v[34:35], v12 offset:18176\n\t"
    "v_mul_f32 v130, s85, v130\n\t"
    "v_mul_f32 v131, s85, v131\n\t"
    "s_waitcnt lgkmcnt(15)\n\t"
    "v_mfma_f32_16x16x32_bf16 v[72:75], v[36:39], v[24:27], 0\n\t"
    "s_mul_i32 s4, s79, 0x38000\n\t"
    "s_add_u32 m0, s35, 0x22400\n\t"
    "s_mov_b64 exec, s[72:73]\n\t"
    "buffer_load_dwordx4 v15, s[44:47], s4 offen lds\n\t"
    "s_mov_b64 exec, -1\n\t"
    "v_mul_f32 v132, s85, v132\n\t"
    "v_mul_f32 v133, s85, v133\n\t"
    "v_mfma_f32_16x16x32_bf16 v[76:79], v[36:39], v[28:31], 0\n\t"
    "ds_read_b64_tr_b16 v[36:37], v12 offset:21888\n\t"
    "ds_read_b64_tr_b16 v[38:39], v12 offset:25472\n\t"
    "v_mul_f32 v134, s85, v134\n\t"
    "v_mul_f32 v135, s85, v135\n\t"
    "s_waitcnt lgkmcnt(15)\n\t"
    "v_mfma_f32_16x16x32_bf16 v[80:83], v[40:43], v[24:27], 0\n\t"
    "v_cvt_pk_bf16_f32 v128, v128, v129\n\t"
    "v_cvt_pk_bf16_f32 v129, v130, v131\n\t"
    "v_mfma_f32_16x16x32_bf16 v[84:87], v[40:43], v[28:31], 0\n\t"
    "ds_read_b64_tr_b16 v[40:41], v12 offset:14624\n\t"
    "ds_read_b64_tr_b16 v[42:43], v12 offset:18208\n\t"
    "v_cvt_pk_bf16_f32 v130, v132, v133\n\t"
    "v_cvt_pk_bf16_f32 v131, v134, v135\n\t"
    "s_waitcnt lgkmcnt(15)\n\t"
    "v_mfma_f32_16x16x32_bf16 v[88:91], v[44:47], v[24:27], 0\n\t"
    "s_mov_b32 s11, 0\n\t"
    "buffer_store_dwordx4 v[128:131], v16, s[52:55], s11 offen sc1\n\t"
    "v_mul_f32 v136, s85, v136\n\t"
    "v_mfma_f32_16x16x32_bf16 v[92:95], v[44:47], v[28:31], 0\n\t"
    "ds_read_b64_tr_b16 v[44:45], v12 offset:21920\n\t"
    "ds_read_b64_tr_b16 v[46:47], v12 offset:25504\n\t"
    "v_mul_f32 v137, s85, v137\n\t"
    "v_mul_f32 v138, s85, v138\n\t"
    "s_waitcnt lgkmcnt(15)\n\t"
    "v_mfma_f32_16x16x32_bf16 v[96:99], v[48:51], v[24:27], 0\n\t"
    "v_mul_f32 v139, s85, v139\n\t"
    "v_mul_f32 v140, s85, v140\n\t"
    "v_mfma_f32_16x16x32_bf16 v[100:103], v[48:51], v[28:31], 0\n\t"
    "ds_read_b64_tr_b16 v[48:49], v12 offset:14656\n\t"
    "ds_read_b64_tr_b16 v[50:51], v12 offset:18240\n\t"
    "v_mul_f32 v141, s85, v141\n\t"
    "v_mul_f32 v142, s85, v142\n\t"
    "s_waitcnt lgkmcnt(15)\n\t"
    "v_mfma_f32_16x16x32_bf16 v[104:107], v[52:55], v[24:27], 0\n\t"
    "s_mul_i32 s4, s79, 0x38000\n\t"
    "s_add_u32 m0, s35, 0x24080\n\t"
    "s_mov_b64 exec, s[72:73]\n\t"
    "buffer_load_dwordx4 v15, s[48:51], s4 offen lds\n\t"
    "s_mov_b64 exec, -1\n\t"
    "v_mul_f32 v143, s85, v143\n\t"
    "v_cvt_pk_bf16_f32 v136, v136, v137\n\t"
    "v_mfma_f32_16x16x32_bf16 v[108:111], v[52:55], v[28:31], 0\n\t"
    "ds_read_b64_tr_b16 v[52:53], v12 offset:21952\n\t"
    "ds_read_b64_tr_b16 v[54:55], v12 offset:25536\n\t"
    "v_cvt_pk_bf16_f32 v137, v138, v139\n\t"
    "v_cvt_pk_bf16_f32 v138, v140, v141\n\t"
    "s_waitcnt lgkmcnt(15)\n\t"
    "v_mfma_f32_16x16x32_bf16 v[112:115], v[56:59], v[24:27], 0\n\t"
    "v_cvt_pk_bf16_f32 v139, v142, v143\n\t"
    "s_mov_b32 s11, 0x10000\n\t"
    "buffer_store_dwordx4 v[136:139], v16, s[52:55], s11 offen sc1\n\t"
    "v_mfma_f32_16x16x32_bf16 v[116:119], v[56:59], v[28:31], 0\n\t"
    "ds_read_b64_tr_b16 v[56:57], v12 offset:14688\n\t"
    "ds_read_b64_tr_b16 v[58:59], v12 offset:18272\n\t"
    "v_mul_f32 v144, s85, v144\n\t"
    "v_mul_f32 v145, s85, v145\n\t"
    "s_waitcnt lgkmcnt(15)\n\t"
    "v_mfma_f32_16x16x32_bf16 v[120:123], v[60:63], v[24:27], 0\n\t"
    "v_mul_f32 v146, s85, v146\n\t"
    "v_mul_f32 v147, s85, v147\n\t"
    "v_mfma_f32_16x16x32_bf16 v[124:127], v[60:63], v[28:31], 0\n\t"
    "ds_read_b64_tr_b16 v[60:61], v12 offset:21984\n\t"
    "ds_read_b64_tr_b16 v[62:63], v12 offset:25568\n\t"
    "s_barrier\n\t"
    "v_mul_f32 v148, s85, v148\n\t"
    "v_mul_f32 v149, s85, v149\n\t"
    "s_waitcnt lgkmcnt(14)\n\t"
    "v_mfma_f32_16x16x32_bf16 v[64:67], v[32:35], v[216:219], v[64:67]\n\t"
    "ds_read_b128 v[24:27], v250\n\t"
    "ds_read_b128 v[28:31], v250 offset:2048\n\t"
    "v_mul_f32 v150, s85, v150\n\t"
    "v_mul_f32 v151, s85, v151\n\t"
    "v_mfma_f32_16x16x32_bf16 v[68:71], v[32:35], v[220:223], v[68:71]\n\t"
    "ds_read_b64_tr_b16 v[32:33], v12 offset:29184\n\t"
    "ds_read_b64_tr_b16 v[34:35], v12 offset:32768\n\t"
    "v_cvt_pk_bf16_f32 v144, v144, v145\n\t"
    "v_cvt_pk_bf16_f32 v145, v146, v147\n\t"
    "s_waitcnt lgkmcnt(15)\n\t"
    "v_mfma_f32_16x16x32_bf16 v[72:75], v[36:39], v[216:219], v[72:75]\n\t"
    "s_mul_i32 s4, s79, 0x38000\n\t"
    "s_add_u32 s4, s4, 0x1c000\n\t"
    "s_add_u32 m0, s35, 0x14000\n\t"
    "s_mov_b64 exec, s[72:73]\n\t"
    "buffer_load_dwordx4 v15, s[44:47], s4 offen lds\n\t"
    "s_mov_b64 exec, -1\n\t"
    "v_cvt_pk_bf16_f32 v146, v148, v149\n\t"
    "v_cvt_pk_bf16_f32 v147, v150, v151\n\t"
    "v_mfma_f32_16x16x32_bf16 v[76:79], v[36:39], v[220:223], v[76:79]\n\t"
    "ds_read_b64_tr_b16 v[36:37], v12 offset:36480\n\t"
    "ds_read_b64_tr_b16 v[38:39], v12 offset:40064\n\t"
    "s_mov_b32 s11, 0x20000\n\t"
    "buffer_store_dwordx4 v[144:147], v16, s[52:55], s11 offen sc1\n\t"
    "v_mul_f32 v152, s85, v152\n\t"
    "s_waitcnt lgkmcnt(15)\n\t"
    "v_mfma_f32_16x16x32_bf16 v[80:83], v[40:43], v[216:219], v[80:83]\n\t"
    "v_mul_f32 v153, s85, v153\n\t"
    "v_mul_f32 v154, s85, v154\n\t"
    "v_mfma_f32_16x16x32_bf16 v[84:87], v[40:43], v[220:223], v[84:87]\n\t"
    "ds_read_b64_tr_b16 v[40:41], v12 offset:29216\n\t"
    "ds_read_b64_tr_b16 v[42:43], v12 offset:32800\n\t"
    "v_mul_f32 v155, s85, v155\n\t"
    "v_mul_f32 v156, s85, v156\n\t"
    "s_waitcnt lgkmcnt(15)\n\t"
    "v_mfma_f32_16x16x32_bf16 v[88:91], v[44:47], v[216:219], v[88:91]\n\t"
    "v_mul_f32 v157, s85, v157\n\t"
    "v_mul_f32 v158, s85, v158\n\t"
    "v_mfma_f32_16x16x32_bf16 v[92:95], v[44:47], v[220:223], v[92:95]\n\t"
    "ds_read_b64_tr_b16 v[44:45], v12 offset:36512\n\t"
    "ds_read_b64_tr_b16 v[46:47], v12 offset:40096\n\t"
    "v_mul_f32 v159, s85, v159\n\t"
    "v_cvt_pk_bf16_f32 v152, v152, v153\n\t"
    "s_waitcnt lgkmcnt(15)\n\t"
    "v_mfma_f32_16x16x32_bf16 v[96:99], v[48:51], v[216:219], v[96:99]\n\t"
    "v_cvt_pk_bf16_f32 v153, v154, v155\n\t"
    "v_cvt_pk_bf16_f32 v154, v156, v157\n\t"
    "v_mfma_f32_16x16x32_bf16 v[100:103], v[48:51], v[220:223], v[100:103]\n\t"
    "ds_read_b64_tr_b16 v[48:49], v12 offset:29248\n\t"
    "ds_read_b64_tr_b16 v[50:51], v12 offset:32832\n\t"
    "v_cvt_pk_bf16_f32 v155, v158, v159\n\t"
    "s_mov_b32 s11, 0x30000\n\t"
    "buffer_store_dwordx4 v[152:155], v16, s[52:55], s11 offen sc1\n\t"
    "s_waitcnt lgkmcnt(15)\n\t"
    "v_mfma_f32_16x16x32_bf16 v[104:107], v[52:55], v[216:219], v[104:107]\n\t"
    "s_mul_i32 s4, s79, 0x38000\n\t"
    "s_add_u32 s4, s4, 0x1c000\n\t"
    "s_add_u32 m0, s35, 0x15c80\n\t"
    "s_mov_b64 exec, s[72:73]\n\t"
    "buffer_load_dwordx4 v15, s[48:51], s4 offen lds\n\t"
    "s_mov_b64 exec, -1\n\t"
    "v_cvt_pk_bf16_f32 v160, v160, v161\n\t"
    "v_cvt_pk_bf16_f32 v161, v162, v163\n\t"
    "v_mfma_f32_16x16x32_bf16 v[108:111], v[52:55], v[220:223], v[108:111]\n\t"
    "ds_read_b64_tr_b16 v[52:53], v12 offset:36544\n\t"
    "ds_read_b64_tr_b16 v[54:55], v12 offset:40128\n\t"
    "v_cvt_pk_bf16_f32 v162, v164, v165\n\t"
    "v_cvt_pk_bf16_f32 v163, v166, v167\n\t"
    "s_waitcnt lgkmcnt(15)\n\t"
    "v_mfma_f32_16x16x32_bf16 v[112:115], v[56:59], v[216:219], v[112:115]\n\t"
    "buffer_store_dwordx4 v[160:163], v17, s[52:55], 0 offen sc1\n\t"
    "v_cvt_pk_bf16_f32 v168, v168, v169\n\t"
    "v_mfma_f32_16x16x32_bf16 v[116:119], v[56:59], v[220:223], v[116:119]\n\t"
    "ds_read_b64_tr_b16 v[56:57], v12 offset:29280\n\t"
    "ds_read_b64_tr_b16 v[58:59], v12 offset:32864\n\t"
    "v_cvt_pk_bf16_f32 v169, v170, v171\n\t"
    "v_cvt_pk_bf16_f32 v170, v172, v173\n\t"
    "s_waitcnt lgkmcnt(15)\n\t"
    "v_mfma_f32_16x16x32_bf16 v[120:123], v[60:63], v[216:219], v[120:123]\n\t"
    "v_cvt_pk_bf16_f32 v171, v174, v175\n\t"
    "buffer_store_dwordx4 v[168:171], v17, s[52:55], 0 offen offset:512 sc1\n\t"
    "v_mfma_f32_16x16x32_bf16 v[124:127], v[60:63], v[220:223], v[124:127]\n\t"
    "ds_read_b64_tr_b16 v[60:61], v12 offset:36576\n\t"
    "ds_read_b64_tr_b16 v[62:63], v12 offset:40160\n\t"
    "s_waitcnt vmcnt(10)\n\t"
    "s_barrier\n\t"
    "s_nop 0\n\t"
    "v_mov_b32 v160, 0\n\t"
    "s_waitcnt lgkmcnt(14)\n\t"
    "v_mfma_f32_16x16x32_bf16 v[64:67], v[32:35], v[24:27], v[64:67]\n\t"
    "ds_read_b128 v[216:219], v251\n\t"
    "ds_read_b128 v[220:223], v251 offset:2048\n\t"
    "v_mov_b32 v161, 0\n\t"
    "v_mov_b32 v162, 0\n\t"
    "v_mfma_f32_16x16x32_bf16 v[68:71], v[32:35], v[28:31], v[68:71]\n\t"
    "ds_read_b64_tr_b16 v[32:33], v255\n\t"
    "ds_read_b64_tr_b16 v[34:35], v255 offset:3584\n\t"
    "v_mov_b32 v163, 0\n\t"
    "v_mov_b32 v164, 0\n\t"
    "s_waitcnt lgkmcnt(15)\n\t"
    "v_mfma_f32_16x16x32_bf16 v[72:75], v[36:39], v[24:27], v[72:75]\n\t"
    "s_mul_i32 s4, s80, 0x38000\n\t"
    "s_add_u32 m0, s35, 0x17900\n\t"
    "s_mov_b64 exec, s[72:73]\n\t"
    "buffer_load_dwordx4 v15, s[44:47], s4 offen lds\n\t"
    "s_mov_b64 exec, -1\n\t"
    "v_mov_b32 v165, 0\n\t"
    "v_mov_b32 v166, 0\n\t"
    "v_mfma_f32_16x16x32_bf16 v[76:79], v[36:39], v[28:31], v[76:79]\n\t"
    "ds_read_b64_tr_b16 v[36:37], v255 offset:7296\n\t"
    "ds_read_b64_tr_b16 v[38:39], v255 offset:10880\n\t"
    "v_mov_b32 v167, 0\n\t"
    "v_mov_b32 v168, 0\n\t"
    "s_waitcnt lgkmcnt(15)\n\t"
    "v_mfma_f32_16x16x32_bf16 v[80:83], v[40:43], v[24:27], v[80:83]\n\t"
    "v_mov_b32 v169, 0\n\t"
    "v_mov_b32 v170, 0\n\t"
    "v_mfma_f32_16x16x32_bf16 v[84:87], v[40:43], v[28:31], v[84:87]\n\t"
    "ds_read_b64_tr_b16 v[40:41], v255 offset:32\n\t"
    "ds_read_b64_tr_b16 v[42:43], v255 offset:3616\n\t"
    "v_mov_b32 v171, 0\n\t"
    "v_mov_b32 v172, 0\n\t"
    "s_waitcnt lgkmcnt(15)\n\t"
    "v_mfma_f32_16x16x32_bf16 v[88:91], v[44:47], v[24:27], v[88:91]\n\t"
    "v_mov_b32 v173, 0\n\t"
    "v_mov_b32 v174, 0\n\t"
    "v_mfma_f32_16x16x32_bf16 v[92:95], v[44:47], v[28:31], v[92:95]\n\t"
    "ds_read_b64_tr_b16 v[44:45], v255 offset:7328\n\t"
    "ds_read_b64_tr_b16 v[46:47], v255 offset:10912\n\t"
    "v_mov_b32 v175, 0\n\t"
    "s_waitcnt lgkmcnt(15)\n\t"
    "v_mfma_f32_16x16x32_bf16 v[96:99], v[48:51], v[24:27], v[96:99]\n\t"
    "v_mfma_f32_16x16x32_bf16 v[100:103], v[48:51], v[28:31], v[100:103]\n\t"
    "ds_read_b64_tr_b16 v[48:49], v255 offset:64\n\t"
    "ds_read_b64_tr_b16 v[50:51], v255 offset:3648\n\t"
    "s_waitcnt lgkmcnt(15)\n\t"
    "v_mfma_f32_16x16x32_bf16 v[104:107], v[52:55], v[24:27], v[104:107]\n\t"
    "s_mul_i32 s4, s80, 0x38000\n\t"
    "s_add_u32 m0, s35, 0x19580\n\t"
    "s_mov_b64 exec, s[72:73]\n\t"
    "buffer_load_dwordx4 v15, s[48:51], s4 offen lds\n\t"
    "s_mov_b64 exec, -1\n\t"
    "v_mfma_f32_16x16x32_bf16 v[108:111], v[52:55], v[28:31], v[108:111]\n\t"
    "ds_read_b64_tr_b16 v[52:53], v255 offset:7360\n\t"
    "ds_read_b64_tr_b16 v[54:55], v255 offset:10944\n\t"
    "s_waitcnt lgkmcnt(15)\n\t"
    "v_mfma_f32_16x16x32_bf16 v[112:115], v[56:59], v[24:27], v[112:115]\n\t"
    "v_mfma_f32_16x16x32_bf16 v[116:119], v[56:59], v[28:31], v[116:119]\n\t"
    "ds_read_b64_tr_b16 v[56:57], v255 offset:96\n\t"
    "ds_read_b64_tr_b16 v[58:59], v255 offset:3680\n\t"
    "s_waitcnt lgkmcnt(15)\n\t"
    "v_mfma_f32_16x16x32_bf16 v[120:123], v[60:63], v[24:27], v[120:123]\n\t"
    "v_mfma_f32_16x16x32_bf16 v[124:127], v[60:63], v[28:31], v[124:127]\n\t"
    "ds_read_b64_tr_b16 v[60:61], v255 offset:7392\n\t"
    "ds_read_b64_tr_b16 v[62:63], v255 offset:10976\n\t"
    "s_waitcnt vmcnt(9)\n\t"
    "s_barrier\n\t"
    "s_waitcnt lgkmcnt(14)\n\t"
    "v_mfma_f32_16x16x32_bf16 v[64:67], v[32:35], v[216:219], v[64:67]\n\t"
    "ds_read_b128 v[24:27], v248\n\t"
    "ds_read_b128 v[28:31], v248 offset:2048\n\t"
    "v_mfma_f32_16x16x32_bf16 v[68:71], v[32:35], v[220:223], v[68:71]\n\t"
    "ds_read_b64_tr_b16 v[32:33], v255 offset:14592\n\t"
    "ds_read_b64_tr_b16 v[34:35], v255 offset:18176\n\t"
    "s_waitcnt lgkmcnt(15)\n\t"
    "v_mfma_f32_16x16x32_bf16 v[72:75], v[36:39], v[216:219], v[72:75]\n\t"
    "s_mul_i32 s4, s80, 0x38000\n\t"
    "s_add_u32 s4, s4, 0x1c000\n\t"
    "s_add_u32 m0, s35, 0x1b200\n\t"
    "s_mov_b64 exec, s[72:73]\n\t"
    "buffer_load_dwordx4 v15, s[44:47], s4 offen lds\n\t"
    "s_mov_b64 exec, -1\n\t"
    "v_mfma_f32_16x16x32_bf16 v[76:79], v[36:39], v[220:223], v[76:79]\n\t"
    "ds_read_b64_tr_b16 v[36:37], v255 offset:21888\n\t"
    "ds_read_b64_tr_b16 v[38:39], v255 offset:25472\n\t"
    "s_waitcnt lgkmcnt(15)\n\t"
    "v_mfma_f32_16x16x32_bf16 v[80:83], v[40:43], v[216:219], v[80:83]\n\t"
    "v_mfma_f32_16x16x32_bf16 v[84:87], v[40:43], v[220:223], v[84:87]\n\t"
    "ds_read_b64_tr_b16 v[40:41], v255 offset:14624\n\t"
    "ds_read_b64_tr_b16 v[42:43], v255 offset:18208\n\t"
    "s_waitcnt lgkmcnt(15)\n\t"
    "v_mfma_f32_16x16x32_bf16 v[88:91], v[44:47], v[216:219], v[88:91]\n\t"
    "v_mfma_f32_16x16x32_bf16 v[92:95], v[44:47], v[220:223], v[92:95]\n\t"
    "ds_read_b64_tr_b16 v[44:45], v255 offset:21920\n\t"
    "ds_read_b64_tr_b16 v[46:47], v255 offset:25504\n\t"
    "s_waitcnt lgkmcnt(15)\n\t"
    "v_mfma_f32_16x16x32_bf16 v[96:99], v[48:51], v[216:219], v[96:99]\n\t"
    "v_mfma_f32_16x16x32_bf16 v[100:103], v[48:51], v[220:223], v[100:103]\n\t"
    "ds_read_b64_tr_b16 v[48:49], v255 offset:14656\n\t"
    "ds_read_b64_tr_b16 v[50:51], v255 offset:18240\n\t"
    "s_waitcnt lgkmcnt(15)\n\t"
    "v_mfma_f32_16x16x32_bf16 v[104:107], v[52:55], v[216:219], v[104:107]\n\t"
    "s_mul_i32 s4, s80, 0x38000\n\t"
    "s_add_u32 s4, s4, 0x1c000\n\t"
    "s_add_u32 m0, s35, 0x1ce80\n\t"
    "s_mov_b64 exec, s[72:73]\n\t"
    "buffer_load_dwordx4 v15, s[48:51], s4 offen lds\n\t"
    "s_mov_b64 exec, -1\n\t"
    "v_mfma_f32_16x16x32_bf16 v[108:111], v[52:55], v[220:223], v[108:111]\n\t"
    "ds_read_b64_tr_b16 v[52:53], v255 offset:21952\n\t"
    "ds_read_b64_tr_b16 v[54:55], v255 offset:25536\n\t"
    "s_waitcnt lgkmcnt(15)\n\t"
    "v_mfma_f32_16x16x32_bf16 v[112:115], v[56:59], v[216:219], v[112:115]\n\t"
    "v_mfma_f32_16x16x32_bf16 v[116:119], v[56:59], v[220:223], v[116:119]\n\t"
    "ds_read_b64_tr_b16 v[56:57], v255 offset:14688\n\t"
    "ds_read_b64_tr_b16 v[58:59], v255 offset:18272\n\t"
    "s_waitcnt lgkmcnt(15)\n\t"
    "v_mfma_f32_16x16x32_bf16 v[120:123], v[60:63], v[216:219], v[120:123]\n\t"
    "v_mfma_f32_16x16x32_bf16 v[124:127], v[60:63], v[220:223], v[124:127]\n\t"
    "ds_read_b64_tr_b16 v[60:61], v255 offset:21984\n\t"
    "ds_read_b64_tr_b16 v[62:63], v255 offset:25568\n\t"
    "s_waitcnt vmcnt(6)\n\t"
    "s_barrier\n\t"
    "s_waitcnt lgkmcnt(14)\n\t"
    "v_mfma_f32_16x16x32_bf16 v[64:67], v[32:35], v[24:27], v[64:67]\n\t"
    "ds_read_b128 v[216:219], v249\n\t"
    "ds_read_b128 v[220:223], v249 offset:2048\n\t"
    "v_mfma_f32_16x16x32_bf16 v[68:71], v[32:35], v[28:31], v[68:71]\n\t"
    "ds_read_b64_tr_b16 v[32:33], v12\n\t"
    "ds_read_b64_tr_b16 v[34:35], v12 offset:3584\n\t"
    "s_waitcnt lgkmcnt(15)\n\t"
    "v_mfma_f32_16x16x32_bf16 v[72:75], v[36:39], v[24:27], v[72:75]\n\t"
    "s_mul_i32 s4, s81, 0x38000\n\t"
    "s_add_u32 m0, s35, 0x1eb00\n\t"
    "s_mov_b64 exec, s[72:73]\n\t"
    "buffer_load_dwordx4 v15, s[44:47], s4 offen lds\n\t"
    "s_mov_b64 exec, -1\n\t"
    "v_mfma_f32_16x16x32_bf16 v[76:79], v[36:39], v[28:31], v[76:79]\n\t"
    "ds_read_b64_tr_b16 v[36:37], v12 offset:7296\n\t"
    "ds_read_b64_tr_b16 v[38:39], v12 offset:10880\n\t"
    "s_waitcnt lgkmcnt(15)\n\t"
    "v_mfma_f32_16x16x32_bf16 v[80:83], v[40:43], v[24:27], v[80:83]\n\t"
    "v_mfma_f32_16x16x32_bf16 v[84:87], v[40:43], v[28:31], v[84:87]\n\t"
    "ds_read_b64_tr_b16 v[40:41], v12 offset:32\n\t"
    "ds_read_b64_tr_b16 v[42:43], v12 offset:3616\n\t"
    "s_waitcnt lgkmcnt(15)\n\t"
    "v_mfma_f32_16x16x32_bf16 v[88:91], v[44:47], v[24:27], v[88:91]\n\t"
    "v_mfma_f32_16x16x32_bf16 v[92:95], v[44:47], v[28:31], v[92:95]\n\t"
    "ds_read_b64_tr_b16 v[44:45], v12 offset:7328\n\t"
    "ds_read_b64_tr_b16 v[46:47], v12 offset:10912\n\t"
    "s_waitcnt lgkmcnt(15)\n\t"
    "v_mfma_f32_16x16x32_bf16 v[96:99], v[48:51], v[24:27], v[96:99]\n\t"
    "v_mfma_f32_16x16x32_bf16 v[100:103], v[48:51], v[28:31], v[100:103]\n\t"
    "ds_read_b64_tr_b16 v[48:49], v12 offset:64\n\t"
    "ds_read_b64_tr_b16 v[50:51], v12 offset:3648\n\t"
    "s_waitcnt lgkmcnt(15)\n\t"
    "v_mfma_f32_16x16x32_bf16 v[104:107], v[52:55], v[24:27], v[104:107]\n\t"
    "s_mul_i32 s4, s81, 0x38000\n\t"
    "s_add_u32 m0, s35, 0x20780\n\t"
    "s_mov_b64 exec, s[72:73]\n\t"
    "buffer_load_dwordx4 v15, s[48:51], s4 offen lds\n\t"
    "s_mov_b64 exec, -1\n\t"
    "v_mfma_f32_16x16x32_bf16 v[108:111], v[52:55], v[28:31], v[108:111]\n\t"
    "ds_read_b64_tr_b16 v[52:53], v12 offset:7360\n\t"
    "ds_read_b64_tr_b16 v[54:55], v12 offset:10944\n\t"
    "s_waitcnt lgkmcnt(15)\n\t"
    "v_mfma_f32_16x16x32_bf16 v[112:115], v[56:59], v[24:27], v[112:115]\n\t"
    "v_mfma_f32_16x16x32_bf16 v[116:119], v[56:59], v[28:31], v[116:119]\n\t"
    "ds_read_b64_tr_b16 v[56:57], v12 offset:96\n\t"
    "ds_read_b64_tr_b16 v[58:59], v12 offset:3680\n\t"
    "s_waitcnt lgkmcnt(15)\n\t"
    "v_mfma_f32_16x16x32_bf16 v[120:123], v[60:63], v[24:27], v[120:123]\n\t"
    "v_mfma_f32_16x16x32_bf16 v[124:127], v[60:63], v[28:31], v[124:127]\n\t"
    "ds_read_b64_tr_b16 v[60:61], v12 offset:7392\n\t"
    "ds_read_b64_tr_b16 v[62:63], v12 offset:10976\n\t"
    "s_waitcnt vmcnt(4)\n\t"
    "s_barrier\n\t"
    "s_cmp_eq_u32 s8, 0\n\t"
    "s_cselect_b64 s[24:25], 1, 0\n\t"
    "s_lshl_b32 s30, s10, 4\n\t"
    "s_add_u32 s30, s30, s9\n\t"
    "s_lshl_b32 s30, s30, 3\n\t"
    "s_add_u32 s30, s30, 0x1004000\n\t"
    "v_mov_b32 v212, s82\n\t"
    "v_mov_b32 v213, s83\n\t"
    "v_mov_b32 v214, s30\n\t"
    "s_mov_b64 exec, s[24:25]\n\t"
    "buffer_store_dwordx2 v[212:213], v214, s[52:55], 0 offen sc1\n\t"
    "s_mov_b64 exec, -1\n\t"
    "s_waitcnt lgkmcnt(14)\n\t"
    "v_mfma_f32_16x16x32_bf16 v[64:67], v[32:35], v[216:219], v[64:67]\n\t"
    "ds_read_b128 v[24:27], v248 offset:16384\n\t"
    "ds_read_b128 v[28:31], v248 offset:18432\n\t"
    "v_mfma_f32_16x16x32_bf16 v[68:71], v[32:35], v[220:223], v[68:71]\n\t"
    "ds_read_b64_tr_b16 v[32:33], v12 offset:14592\n\t"
    "ds_read_b64_tr_b16 v[34:35], v12 offset:18176\n\t"
    "s_waitcnt lgkmcnt(15)\n\t"
    "v_mfma_f32_16x16x32_bf16 v[72:75], v[36:39], v[216:219], v[72:75]\n\t"
    "s_mul_i32 s4, s81, 0x38000\n\t"
    "s_add_u32 s4, s4, 0x1c000\n\t"
    "s_add_u32 m0, s35, 0x22400\n\t"
    "s_mov_b64 exec, s[72:73]\n\t"
    "buffer_load_dwordx4 v15, s[44:47], s4 offen lds\n\t"
    "s_mov_b64 exec, -1\n\t"
    "v_mfma_f32_16x16x32_bf16 v[76:79], v[36:39], v[220:223], v[76:79]\n\t"
    "ds_read_b64_tr_b16 v[36:37], v12 offset:21888\n\t"
    "ds_read_b64_tr_b16 v[38:39], v12 offset:25472\n\t"
    "s_waitcnt lgkmcnt(15)\n\t"
    "v_mfma_f32_16x16x32_bf16 v[80:83], v[40:43], v[216:219], v[80:83]\n\t"
    "v_mfma_f32_16x16x32_bf16 v[84:87], v[40:43], v[220:223], v[84:87]\n\t"
    "ds_read_b64_tr_b16 v[40:41], v12 offset:14624\n\t"
    "ds_read_b64_tr_b16 v[42:43], v12 offset:18208\n\t"
    "s_waitcnt lgkmcnt(15)\n\t"
    "v_mfma_f32_16x16x32_bf16 v[88:91], v[44:47], v[216:219], v[88:91]\n\t"
    "v_mfma_f32_16x16x32_bf16 v[92:95], v[44:47], v[220:223], v[92:95]\n\t"
    "ds_read_b64_tr_b16 v[44:45], v12 offset:21920\n\t"
    "ds_read_b64_tr_b16 v[46:47], v12 offset:25504\n\t"
    "s_waitcnt lgkmcnt(15)\n\t"
    "v_mfma_f32_16x16x32_bf16 v[96:99], v[48:51], v[216:219], v[96:99]\n\t"
    "v_mfma_f32_16x16x32_bf16 v[100:103], v[48:51], v[220:223], v[100:103]\n\t"
    "ds_read_b64_tr_b16 v[48:49], v12 offset:14656\n\t"
    "ds_read_b64_tr_b16 v[50:51], v12 offset:18240\n\t"
    "s_waitcnt lgkmcnt(15)\n\t"
    "v_mfma_f32_16x16x32_bf16 v[104:107], v[52:55], v[216:219], v[104:107]\n\t"
    "s_mul_i32 s4, s81, 0x38000\n\t"
    "s_add_u32 s4, s4, 0x1c000\n\t"
    "s_add_u32 m0, s35, 0x24080\n\t"
    "s_mov_b64 exec, s[72:73]\n\t"
    "buffer_load_dwordx4 v15, s[48:51], s4 offen lds\n\t"
    "s_mov_b64 exec, -1\n\t"
    "v_mfma_f32_16x16x32_bf16 v[108:111], v[52:55], v[220:223], v[108:111]\n\t"
    "ds_read_b64_tr_b16 v[52:53], v12 offset:21952\n\t"
    "ds_read_b64_tr_b16 v[54:55], v12 offset:25536\n\t"
    "s_waitcnt lgkmcnt(15)\n\t"
    "v_mfma_f32_16x16x32_bf16 v[112:115], v[56:59], v[216:219], v[112:115]\n\t"
    "v_mfma_f32_16x16x32_bf16 v[116:119], v[56:59], v[220:223], v[116:119]\n\t"
    "ds_read_b64_tr_b16 v[56:57], v12 offset:14688\n\t"
    "ds_read_b64_tr_b16 v[58:59], v12 offset:18272\n\t"
    "s_waitcnt lgkmcnt(15)\n\t"
    "v_mfma_f32_16x16x32_bf16 v[120:123], v[60:63], v[216:219], v[120:123]\n\t"
    "v_mfma_f32_16x16x32_bf16 v[124:127], v[60:63], v[220:223], v[124:127]\n\t"
    "ds_read_b64_tr_b16 v[60:61], v12 offset:21984\n\t"
    "ds_read_b64_tr_b16 v[62:63], v12 offset:25568\n\t"
    "s_waitcnt vmcnt(4)\n\t"
    "s_barrier\n\t"
    "s_waitcnt lgkmcnt(14)\n\t"
    "v_mfma_f32_16x16x32_bf16 v[64:67], v[32:35], v[24:27], v[64:67]\n\t"
    "ds_read_b128 v[216:219], v249 offset:16384\n\t"
    "ds_read_b128 v[220:223], v249 offset:18432\n\t"
    "v_mfma_f32_16x16x32_bf16 v[68:71], v[32:35], v[28:31], v[68:71]\n\t"
    "ds_read_b64_tr_b16 v[32:33], v12 offset:29184\n\t"
    "ds_read_b64_tr_b16 v[34:35], v12 offset:32768\n\t"
    "s_waitcnt lgkmcnt(15)\n\t"
    "v_mfma_f32_16x16x32_bf16 v[72:75], v[36:39], v[24:27], v[72:75]\n\t"
    "s_mul_i32 s4, s74, 0x38000\n\t"
    "s_add_u32 m0, s35, 0x14000\n\t"
    "s_mov_b64 exec, s[72:73]\n\t"
    "buffer_load_dwordx4 v15, s[44:47], s4 offen lds\n\t"
    "s_mov_b64 exec, -1\n\t"
    "v_mfma_f32_16x16x32_bf16 v[76:79], v[36:39], v[28:31], v[76:79]\n\t"
    "ds_read_b64_tr_b16 v[36:37], v12 offset:36480\n\t"
    "ds_read_b64_tr_b16 v[38:39], v12 offset:40064\n\t"
    "s_waitcnt lgkmcnt(15)\n\t"
    "v_mfma_f32_16x16x32_bf16 v[80:83], v[40:43], v[24:27], v[80:83]\n\t"
    "v_mfma_f32_16x16x32_bf16 v[84:87], v[40:43], v[28:31], v[84:87]\n\t"
    "ds_read_b64_tr_b16 v[40:41], v12 offset:29216\n\t"
    "ds_read_b64_tr_b16 v[42:43], v12 offset:32800\n\t"
    "s_waitcnt lgkmcnt(15)\n\t"
    "v_mfma_f32_16x16x32_bf16 v[88:91], v[44:47], v[24:27], v[88:91]\n\t"
    "v_mfma_f32_16x16x32_bf16 v[92:95], v[44:47], v[28:31], v[92:95]\n\t"
    "ds_read_b64_tr_b16 v[44:45], v12 offset:36512\n\t"
    "ds_read_b64_tr_b16 v[46:47], v12 offset:40096\n\t"
    "s_waitcnt lgkmcnt(15)\n\t"
    "v_mfma_f32_16x16x32_bf16 v[96:99], v[48:51], v[24:27], v[96:99]\n\t"
    "v_mfma_f32_16x16x32_bf16 v[100:103], v[48:51], v[28:31], v[100:103]\n\t"
    "ds_read_b64_tr_b16 v[48:49], v12 offset:29248\n\t"
    "ds_read_b64_tr_b16 v[50:51], v12 offset:32832\n\t"
    "s_waitcnt lgkmcnt(15)\n\t"
    "v_mfma_f32_16x16x32_bf16 v[104:107], v[52:55], v[24:27], v[104:107]\n\t"
    "s_mul_i32 s4, s74, 0x38000\n\t"
    "s_add_u32 m0, s35, 0x15c80\n\t"
    "s_mov_b64 exec, s[72:73]\n\t"
    "buffer_load_dwordx4 v15, s[48:51], s4 offen lds\n\t"
    "s_mov_b64 exec, -1\n\t"
    "v_mfma_f32_16x16x32_bf16 v[108:111], v[52:55], v[28:31], v[108:111]\n\t"
    "ds_read_b64_tr_b16 v[52:53], v12 offset:36544\n\t"
    "ds_read_b64_tr_b16 v[54:55], v12 offset:40128\n\t"
    "s_waitcnt lgkmcnt(15)\n\t"
    "v_mfma_f32_16x16x32_bf16 v[112:115], v[56:59], v[24:27], v[112:115]\n\t"
    "v_mfma_f32_16x16x32_bf16 v[116:119], v[56:59], v[28:31], v[116:119]\n\t"
    "ds_read_b64_tr_b16 v[56:57], v12 offset:29280\n\t"
    "ds_read_b64_tr_b16 v[58:59], v12 offset:32864\n\t"
    "s_waitcnt lgkmcnt(15)\n\t"
    "v_mfma_f32_16x16x32_bf16 v[120:123], v[60:63], v[24:27], v[120:123]\n\t"
    "v_mfma_f32_16x16x32_bf16 v[124:127], v[60:63], v[28:31], v[124:127]\n\t"
    "ds_read_b64_tr_b16 v[60:61], v12 offset:36576\n\t"
    "ds_read_b64_tr_b16 v[62:63], v12 offset:40160\n\t"
    "s_waitcnt vmcnt(4)\n\t"
    "s_barrier\n\t"
    "s_waitcnt lgkmcnt(14)\n\t"
    "v_mfma_f32_16x16x32_bf16 v[64:67], v[32:35], v[216:219], v[64:67]\n\t"
    "ds_read_b128 v[24:27], v248 offset:32768\n\t"
    "ds_read_b128 v[28:31], v248 offset:34816\n\t"
    "v_mfma_f32_16x16x32_bf16 v[68:71], v[32:35], v[220:223], v[68:71]\n\t"
    "ds_read_b64_tr_b16 v[32:33], v255\n\t"
    "ds_read_b64_tr_b16 v[34:35], v255 offset:3584\n\t"
    "s_waitcnt lgkmcnt(15)\n\t"
    "v_mfma_f32_16x16x32_bf16 v[72:75], v[36:39], v[216:219], v[72:75]\n\t"
    "s_mul_i32 s4, s74, 0x38000\n\t"
    "s_add_u32 s4, s4, 0x1c000\n\t"
    "s_add_u32 m0, s35, 0x17900\n\t"
    "s_mov_b64 exec, s[72:73]\n\t"
    "buffer_load_dwordx4 v15, s[44:47], s4 offen lds\n\t"
    "s_mov_b64 exec, -1\n\t"
    "v_mfma_f32_16x16x32_bf16 v[76:79], v[36:39], v[220:223], v[76:79]\n\t"
    "ds_read_b64_tr_b16 v[36:37], v255 offset:7296\n\t"
    "ds_read_b64_tr_b16 v[38:39], v255 offset:10880\n\t"
    "s_waitcnt lgkmcnt(15)\n\t"
    "v_mfma_f32_16x16x32_bf16 v[80:83], v[40:43], v[216:219], v[80:83]\n\t"
    "v_mfma_f32_16x16x32_bf16 v[84:87], v[40:43], v[220:223], v[84:87]\n\t"
    "ds_read_b64_tr_b16 v[40:41], v255 offset:32\n\t"
    "ds_read_b64_tr_b16 v[42:43], v255 offset:3616\n\t"
    "s_waitcnt lgkmcnt(15)\n\t"
    "v_mfma_f32_16x16x32_bf16 v[88:91], v[44:47], v[216:219], v[88:91]\n\t"
    "v_mfma_f32_16x16x32_bf16 v[92:95], v[44:47], v[220:223], v[92:95]\n\t"
    "ds_read_b64_tr_b16 v[44:45], v255 offset:7328\n\t"
    "ds_read_b64_tr_b16 v[46:47], v255 offset:10912\n\t"
    "s_waitcnt lgkmcnt(15)\n\t"
    "v_mfma_f32_16x16x32_bf16 v[96:99], v[48:51], v[216:219], v[96:99]\n\t"
    "v_mfma_f32_16x16x32_bf16 v[100:103], v[48:51], v[220:223], v[100:103]\n\t"
    "ds_read_b64_tr_b16 v[48:49], v255 offset:64\n\t"
    "ds_read_b64_tr_b16 v[50:51], v255 offset:3648\n\t"
    "s_waitcnt lgkmcnt(15)\n\t"
    "v_mfma_f32_16x16x32_bf16 v[104:107], v[52:55], v[216:219], v[104:107]\n\t"
    "s_mul_i32 s4, s74, 0x38000\n\t"
    "s_add_u32 s4, s4, 0x1c000\n\t"
    "s_add_u32 m0, s35, 0x19580\n\t"
    "s_mov_b64 exec, s[72:73]\n\t"
    "buffer_load_dwordx4 v15, s[48:51], s4 offen lds\n\t"
    "s_mov_b64 exec, -1\n\t"
    "v_mfma_f32_16x16x32_bf16 v[108:111], v[52:55], v[220:223], v[108:111]\n\t"
    "ds_read_b64_tr_b16 v[52:53], v255 offset:7360\n\t"
    "ds_read_b64_tr_b16 v[54:55], v255 offset:10944\n\t"
    "s_waitcnt lgkmcnt(15)\n\t"
    "v_mfma_f32_16x16x32_bf16 v[112:115], v[56:59], v[216:219], v[112:115]\n\t"
    "v_mfma_f32_16x16x32_bf16 v[116:119], v[56:59], v[220:223], v[116:119]\n\t"
    "ds_read_b64_tr_b16 v[56:57], v255 offset:96\n\t"
    "ds_read_b64_tr_b16 v[58:59], v255 offset:3680\n\t"
    "s_waitcnt lgkmcnt(15)\n\t"
    "v_mfma_f32_16x16x32_bf16 v[120:123], v[60:63], v[216:219], v[120:123]\n\t"
    "v_mfma_f32_16x16x32_bf16 v[124:127], v[60:63], v[220:223], v[124:127]\n\t"
    "ds_read_b64_tr_b16 v[60:61], v255 offset:7392\n\t"
    "ds_read_b64_tr_b16 v[62:63], v255 offset:10976\n\t"
    "s_waitcnt vmcnt(4)\n\t"
    "s_barrier\n\t"
    "s_waitcnt lgkmcnt(14)\n\t"
    "v_mfma_f32_16x16x32_bf16 v[64:67], v[32:35], v[24:27], v[64:67]\n\t"
    "ds_read_b128 v[216:219], v249 offset:32768\n\t"
    "ds_read_b128 v[220:223], v249 offset:34816\n\t"
    "v_mfma_f32_16x16x32_bf16 v[68:71], v[32:35], v[28:31], v[68:71]\n\t"
    "ds_read_b64_tr_b16 v[32:33], v255 offset:14592\n\t"
    "ds_read_b64_tr_b16 v[34:35], v255 offset:18176\n\t"
    "s_waitcnt lgkmcnt(15)\n\t"
    "v_mfma_f32_16x16x32_bf16 v[72:75], v[36:39], v[24:27], v[72:75]\n\t"
    "s_mul_i32 s4, s75, 0x38000\n\t"
    "s_add_u32 m0, s35, 0x1b200\n\t"
    "s_mov_b64 exec, s[72:73]\n\t"
    "buffer_load_dwordx4 v15, s[44:47], s4 offen lds\n\t"
    "s_mov_b64 exec, -1\n\t"
    "v_mfma_f32_16x16x32_bf16 v[76:79], v[36:39], v[28:31], v[76:79]\n\t"
    "ds_read_b64_tr_b16 v[36:37], v255 offset:21888\n\t"
    "ds_read_b64_tr_b16 v[38:39], v255 offset:25472\n\t"
    "s_waitcnt lgkmcnt(15)\n\t"
    "v_mfma_f32_16x16x32_bf16 v[80:83], v[40:43], v[24:27], v[80:83]\n\t"
    "v_mfma_f32_16x16x32_bf16 v[84:87], v[40:43], v[28:31], v[84:87]\n\t"
    "ds_read_b64_tr_b16 v[40:41], v255 offset:14624\n\t"
    "ds_read_b64_tr_b16 v[42:43], v255 offset:18208\n\t"
    "s_waitcnt lgkmcnt(15)\n\t"
    "v_mfma_f32_16x16x32_bf16 v[88:91], v[44:47], v[24:27], v[88:91]\n\t"
    "v_mfma_f32_16x16x32_bf16 v[92:95], v[44:47], v[28:31], v[92:95]\n\t"
    "ds_read_b64_tr_b16 v[44:45], v255 offset:21920\n\t"
    "ds_read_b64_tr_b16 v[46:47], v255 offset:25504\n\t"
    "s_waitcnt lgkmcnt(15)\n\t"
    "v_mfma_f32_16x16x32_bf16 v[96:99], v[48:51], v[24:27], v[96:99]\n\t"
    "v_mfma_f32_16x16x32_bf16 v[100:103], v[48:51], v[28:31], v[100:103]\n\t"
    "ds_read_b64_tr_b16 v[48:49], v255 offset:14656\n\t"
    "ds_read_b64_tr_b16 v[50:51], v255 offset:18240\n\t"
    "s_waitcnt lgkmcnt(15)\n\t"
    "v_mfma_f32_16x16x32_bf16 v[104:107], v[52:55], v[24:27], v[104:107]\n\t"
    "s_mul_i32 s4, s75, 0x38000\n\t"
    "s_add_u32 m0, s35, 0x1ce80\n\t"
    "s_mov_b64 exec, s[72:73]\n\t"
    "buffer_load_dwordx4 v15, s[48:51], s4 offen lds\n\t"
    "s_mov_b64 exec, -1\n\t"
    "v_mfma_f32_16x16x32_bf16 v[108:111], v[52:55], v[28:31], v[108:111]\n\t"
    "ds_read_b64_tr_b16 v[52:53], v255 offset:21952\n\t"
    "ds_read_b64_tr_b16 v[54:55], v255 offset:25536\n\t"
    "s_waitcnt lgkmcnt(15)\n\t"
    "v_mfma_f32_16x16x32_bf16 v[112:115], v[56:59], v[24:27], v[112:115]\n\t"
    "v_mfma_f32_16x16x32_bf16 v[116:119], v[56:59], v[28:31], v[116:119]\n\t"
    "ds_read_b64_tr_b16 v[56:57], v255 offset:14688\n\t"
    "ds_read_b64_tr_b16 v[58:59], v255 offset:18272\n\t"
    "s_waitcnt lgkmcnt(15)\n\t"
    "v_mfma_f32_16x16x32_bf16 v[120:123], v[60:63], v[24:27], v[120:123]\n\t"
    "v_mfma_f32_16x16x32_bf16 v[124:127], v[60:63], v[28:31], v[124:127]\n\t"
    "ds_read_b64_tr_b16 v[60:61], v255 offset:21984\n\t"
    "ds_read_b64_tr_b16 v[62:63], v255 offset:25568\n\t"
    "s_waitcnt vmcnt(4)\n\t"
    "s_barrier\n\t"
    "s_waitcnt lgkmcnt(14)\n\t"
    "v_mfma_f32_16x16x32_bf16 v[64:67], v[32:35], v[216:219], v[64:67]\n\t"
    "ds_read_b128 v[24:27], v248 offset:49152\n\t"
    "ds_read_b128 v[28:31], v248 offset:51200\n\t"
    "v_mfma_f32_16x16x32_bf16 v[68:71], v[32:35], v[220:223], v[68:71]\n\t"
    "ds_read_b64_tr_b16 v[32:33], v12\n\t"
    "ds_read_b64_tr_b16 v[34:35], v12 offset:3584\n\t"
    "s_waitcnt lgkmcnt(15)\n\t"
    "v_mfma_f32_16x16x32_bf16 v[72:75], v[36:39], v[216:219], v[72:75]\n\t"
    "s_mul_i32 s4, s75, 0x38000\n\t"
    "s_add_u32 s4, s4, 0x1c000\n\t"
    "s_add_u32 m0, s35, 0x1eb00\n\t"
    "s_mov_b64 exec, s[72:73]\n\t"
    "buffer_load_dwordx4 v15, s[44:47], s4 offen lds\n\t"
    "s_mov_b64 exec, -1\n\t"
    "v_mfma_f32_16x16x32_bf16 v[76:79], v[36:39], v[220:223], v[76:79]\n\t"
    "ds_read_b64_tr_b16 v[36:37], v12 offset:7296\n\t"
    "ds_read_b64_tr_b16 v[38:39], v12 offset:10880\n\t"
    "s_waitcnt lgkmcnt(15)\n\t"
    "v_mfma_f32_16x16x32_bf16 v[80:83], v[40:43], v[216:219], v[80:83]\n\t"
    "v_mfma_f32_16x16x32_bf16 v[84:87], v[40:43], v[220:223], v[84:87]\n\t"
    "ds_read_b64_tr_b16 v[40:41], v12 offset:32\n\t"
    "ds_read_b64_tr_b16 v[42:43], v12 offset:3616\n\t"
    "s_waitcnt lgkmcnt(15)\n\t"
    "v_mfma_f32_16x16x32_bf16 v[88:91], v[44:47], v[216:219], v[88:91]\n\t"
    "v_mfma_f32_16x16x32_bf16 v[92:95], v[44:47], v[220:223], v[92:95]\n\t"
    "ds_read_b64_tr_b16 v[44:45], v12 offset:7328\n\t"
    "ds_read_b64_tr_b16 v[46:47], v12 offset:10912\n\t"
    "s_waitcnt lgkmcnt(15)\n\t"
    "v_mfma_f32_16x16x32_bf16 v[96:99], v[48:51], v[216:219], v[96:99]\n\t"
    "v_mfma_f32_16x16x32_bf16 v[100:103], v[48:51], v[220:223], v[100:103]\n\t"
    "ds_read_b64_tr_b16 v[48:49], v12 offset:64\n\t"
    "ds_read_b64_tr_b16 v[50:51], v12 offset:3648\n\t"
    "s_waitcnt lgkmcnt(15)\n\t"
    "v_mfma_f32_16x16x32_bf16 v[104:107], v[52:55], v[216:219], v[104:107]\n\t"
    "s_mul_i32 s4, s75, 0x38000\n\t"
    "s_add_u32 s4, s4, 0x1c000\n\t"
    "s_add_u32 m0, s35, 0x20780\n\t"
    "s_mov_b64 exec, s[72:73]\n\t"
    "buffer_load_dwordx4 v15, s[48:51], s4 offen lds\n\t"
    "s_mov_b64 exec, -1\n\t"
    "v_mfma_f32_16x16x32_bf16 v[108:111], v[52:55], v[220:223], v[108:111]\n\t"
    "ds_read_b64_tr_b16 v[52:53], v12 offset:7360\n\t"
    "ds_read_b64_tr_b16 v[54:55], v12 offset:10944\n\t"
    "s_waitcnt lgkmcnt(15)\n\t"
    "v_mfma_f32_16x16x32_bf16 v[112:115], v[56:59], v[216:219], v[112:115]\n\t"
    "v_mfma_f32_16x16x32_bf16 v[116:119], v[56:59], v[220:223], v[116:119]\n\t"
    "ds_read_b64_tr_b16 v[56:57], v12 offset:96\n\t"
    "ds_read_b64_tr_b16 v[58:59], v12 offset:3680\n\t"
    "s_waitcnt lgkmcnt(15)\n\t"
    "v_mfma_f32_16x16x32_bf16 v[120:123], v[60:63], v[216:219], v[120:123]\n\t"
    "v_mfma_f32_16x16x32_bf16 v[124:127], v[60:63], v[220:223], v[124:127]\n\t"
    "ds_read_b64_tr_b16 v[60:61], v12 offset:7392\n\t"
    "ds_read_b64_tr_b16 v[62:63], v12 offset:10976\n\t"
    "s_waitcnt vmcnt(4)\n\t"
    "s_barrier\n\t"
    "s_waitcnt lgkmcnt(14)\n\t"
    "v_mfma_f32_16x16x32_bf16 v[64:67], v[32:35], v[24:27], v[64:67]\n\t"
    "ds_read_b128 v[216:219], v249 offset:49152\n\t"
    "ds_read_b128 v[220:223], v249 offset:51200\n\t"
    "v_mfma_f32_16x16x32_bf16 v[68:71], v[32:35], v[28:31], v[68:71]\n\t"
    "ds_read_b64_tr_b16 v[32:33], v12 offset:14592\n\t"
    "ds_read_b64_tr_b16 v[34:35], v12 offset:18176\n\t"
    "s_waitcnt lgkmcnt(15)\n\t"
    "v_mfma_f32_16x16x32_bf16 v[72:75], v[36:39], v[24:27], v[72:75]\n\t"
    "s_mul_i32 s4, s76, 0x38000\n\t"
    "s_add_u32 m0, s35, 0x22400\n\t"
    "s_mov_b64 exec, s[72:73]\n\t"
    "buffer_load_dwordx4 v15, s[44:47], s4 offen lds\n\t"
    "s_mov_b64 exec, -1\n\t"
    "v_mfma_f32_16x16x32_bf16 v[76:79], v[36:39], v[28:31], v[76:79]\n\t"
    "ds_read_b64_tr_b16 v[36:37], v12 offset:21888\n\t"
    "ds_read_b64_tr_b16 v[38:39], v12 offset:25472\n\t"
    "s_waitcnt lgkmcnt(15)\n\t"
    "v_mfma_f32_16x16x32_bf16 v[80:83], v[40:43], v[24:27], v[80:83]\n\t"
    "v_mfma_f32_16x16x32_bf16 v[84:87], v[40:43], v[28:31], v[84:87]\n\t"
    "ds_read_b64_tr_b16 v[40:41], v12 offset:14624\n\t"
    "ds_read_b64_tr_b16 v[42:43], v12 offset:18208\n\t"
    "s_waitcnt lgkmcnt(15)\n\t"
    "v_mfma_f32_16x16x32_bf16 v[88:91], v[44:47], v[24:27], v[88:91]\n\t"
    "v_mfma_f32_16x16x32_bf16 v[92:95], v[44:47], v[28:31], v[92:95]\n\t"
    "ds_read_b64_tr_b16 v[44:45], v12 offset:21920\n\t"
    "ds_read_b64_tr_b16 v[46:47], v12 offset:25504\n\t"
    "s_waitcnt lgkmcnt(15)\n\t"
    "v_mfma_f32_16x16x32_bf16 v[96:99], v[48:51], v[24:27], v[96:99]\n\t"
    "v_mfma_f32_16x16x32_bf16 v[100:103], v[48:51], v[28:31], v[100:103]\n\t"
    "ds_read_b64_tr_b16 v[48:49], v12 offset:14656\n\t"
    "ds_read_b64_tr_b16 v[50:51], v12 offset:18240\n\t"
    "s_waitcnt lgkmcnt(15)\n\t"
    "v_mfma_f32_16x16x32_bf16 v[104:107], v[52:55], v[24:27], v[104:107]\n\t"
    "s_mul_i32 s4, s76, 0x38000\n\t"
    "s_add_u32 m0, s35, 0x24080\n\t"
    "s_mov_b64 exec, s[72:73]\n\t"
    "buffer_load_dwordx4 v15, s[48:51], s4 offen lds\n\t"
    "s_mov_b64 exec, -1\n\t"
    "v_mfma_f32_16x16x32_bf16 v[108:111], v[52:55], v[28:31], v[108:111]\n\t"
    "ds_read_b64_tr_b16 v[52:53], v12 offset:21952\n\t"
    "ds_read_b64_tr_b16 v[54:55], v12 offset:25536\n\t"
    "s_waitcnt lgkmcnt(15)\n\t"
    "v_mfma_f32_16x16x32_bf16 v[112:115], v[56:59], v[24:27], v[112:115]\n\t"
    "v_mfma_f32_16x16x32_bf16 v[116:119], v[56:59], v[28:31], v[116:119]\n\t"
    "ds_read_b64_tr_b16 v[56:57], v12 offset:14688\n\t"
    "ds_read_b64_tr_b16 v[58:59], v12 offset:18272\n\t"
    "s_waitcnt lgkmcnt(15)\n\t"
    "v_mfma_f32_16x16x32_bf16 v[120:123], v[60:63], v[24:27], v[120:123]\n\t"
    "v_mfma_f32_16x16x32_bf16 v[124:127], v[60:63], v[28:31], v[124:127]\n\t"
    "ds_read_b64_tr_b16 v[60:61], v12 offset:21984\n\t"
    "ds_read_b64_tr_b16 v[62:63], v12 offset:25568\n\t"
    "s_waitcnt vmcnt(4)\n\t"
    "s_barrier\n\t"
    "s_waitcnt lgkmcnt(14)\n\t"
    "v_mfma_f32_16x16x32_bf16 v[64:67], v[32:35], v[216:219], v[64:67]\n\t"
    "ds_read_b128 v[24:27], v250\n\t"
    "ds_read_b128 v[28:31], v250 offset:2048\n\t"
    "v_mfma_f32_16x16x32_bf16 v[68:71], v[32:35], v[220:223], v[68:71]\n\t"
    "ds_read_b64_tr_b16 v[32:33], v12 offset:29184\n\t"
    "ds_read_b64_tr_b16 v[34:35], v12 offset:32768\n\t"
    "s_waitcnt lgkmcnt(15)\n\t"
    "v_mfma_f32_16x16x32_bf16 v[72:75], v[36:39], v[216:219], v[72:75]\n\t"
    "s_mul_i32 s4, s76, 0x38000\n\t"
    "s_add_u32 s4, s4, 0x1c000\n\t"
    "s_add_u32 m0, s35, 0x14000\n\t"
    "s_mov_b64 exec, s[72:73]\n\t"
    "buffer_load_dwordx4 v15, s[44:47], s4 offen lds\n\t"
    "s_mov_b64 exec, -1\n\t"
    "v_mfma_f32_16x16x32_bf16 v[76:79], v[36:39], v[220:223], v[76:79]\n\t"
    "ds_read_b64_tr_b16 v[36:37], v12 offset:36480\n\t"
    "ds_read_b64_tr_b16 v[38:39], v12 offset:40064\n\t"
    "s_waitcnt lgkmcnt(15)\n\t"
    "v_mfma_f32_16x16x32_bf16 v[80:83], v[40:43], v[216:219], v[80:83]\n\t"
    "v_mfma_f32_16x16x32_bf16 v[84:87], v[40:43], v[220:223], v[84:87]\n\t"
    "ds_read_b64_tr_b16 v[40:41], v12 offset:29216\n\t"
    "ds_read_b64_tr_b16 v[42:43], v12 offset:32800\n\t"
    "s_waitcnt lgkmcnt(15)\n\t"
    "v_mfma_f32_16x16x32_bf16 v[88:91], v[44:47], v[216:219], v[88:91]\n\t"
    "s_add_u32 m0, s16, 0x4000\n\t"
    "s_mov_b32 s4, 0x200000\n\t"
    "buffer_load_dwordx4 v199, s[52:55], s4 offen sc1 lds\n\t"
    "v_mfma_f32_16x16x32_bf16 v[92:95], v[44:47], v[220:223], v[92:95]\n\t"
    "ds_read_b64_tr_b16 v[44:45], v12 offset:36512\n\t"
    "ds_read_b64_tr_b16 v[46:47], v12 offset:40096\n\t"
    "s_waitcnt lgkmcnt(15)\n\t"
    "v_mfma_f32_16x16x32_bf16 v[96:99], v[48:51], v[216:219], v[96:99]\n\t"
    "v_mfma_f32_16x16x32_bf16 v[100:103], v[48:51], v[220:223], v[100:103]\n\t"
    "ds_read_b64_tr_b16 v[48:49], v12 offset:29248\n\t"
    "ds_read_b64_tr_b16 v[50:51], v12 offset:32832\n\t"
    "s_waitcnt lgkmcnt(15)\n\t"
    "v_mfma_f32_16x16x32_bf16 v[104:107], v[52:55], v[216:219], v[104:107]\n\t"
    "s_mul_i32 s4, s76, 0x38000\n\t"
    "s_add_u32 s4, s4, 0x1c000\n\t"
    "s_add_u32 m0, s35, 0x15c80\n\t"
    "s_mov_b64 exec, s[72:73]\n\t"
    "buffer_load_dwordx4 v15, s[48:51], s4 offen lds\n\t"
    "s_mov_b64 exec, -1\n\t"
    "v_mfma_f32_16x16x32_bf16 v[108:111], v[52:55], v[220:223], v[108:111]\n\t"
    "ds_read_b64_tr_b16 v[52:53], v12 offset:36544\n\t"
    "ds_read_b64_tr_b16 v[54:55], v12 offset:40128\n\t"
    "s_waitcnt lgkmcnt(15)\n\t"
    "v_mfma_f32_16x16x32_bf16 v[112:115], v[56:59], v[216:219], v[112:115]\n\t"
    "s_add_u32 m0, s16, 0x4000\n\t"
    "s_mov_b32 s4, 0x200000\n\t"
    "buffer_load_dwordx4 v199, s[52:55], s4 offen offset:1024 sc1 lds\n\t"
    "v_mfma_f32_16x16x32_bf16 v[116:119], v[56:59], v[220:223], v[116:119]\n\t"
    "ds_read_b64_tr_b16 v[56:57], v12 offset:29280\n\t"
    "ds_read_b64_tr_b16 v[58:59], v12 offset:32864\n\t"
    "s_waitcnt lgkmcnt(15)\n\t"
    "v_mfma_f32_16x16x32_bf16 v[120:123], v[60:63], v[216:219], v[120:123]\n\t"
    "s_add_u32 m0, s16, 0x8000\n\t"
    "s_mov_b32 s4, 0x400000\n\t"
    "buffer_load_dwordx4 v199, s[52:55], s4 offen sc1 lds\n\t"
    "v_mfma_f32_16x16x32_bf16 v[124:127], v[60:63], v[220:223], v[124:127]\n\t"
    "ds_read_b64_tr_b16 v[60:61], v12 offset:36576\n\t"
    "ds_read_b64_tr_b16 v[62:63], v12 offset:40160\n\t"
    "s_waitcnt vmcnt(7)\n\t"
    "s_barrier\n\t"
    "s_waitcnt lgkmcnt(14)\n\t"
    "v_mfma_f32_16x16x32_bf16 v[64:67], v[32:35], v[24:27], v[64:67]\n\t"
    "ds_read_b128 v[216:219], v251\n\t"
    "ds_read_b128 v[220:223], v251 offset:2048\n\t"
    "v_mfma_f32_16x16x32_bf16 v[68:71], v[32:35], v[28:31], v[68:71]\n\t"
    "ds_read_b64_tr_b16 v[32:33], v255\n\t"
    "ds_read_b64_tr_b16 v[34:35], v255 offset:3584\n\t"
    "s_waitcnt lgkmcnt(15)\n\t"
    "v_mfma_f32_16x16x32_bf16 v[72:75], v[36:39], v[24:27], v[72:75]\n\t"
    "v_mfma_f32_16x16x32_bf16 v[76:79], v[36:39], v[28:31], v[76:79]\n\t"
    "ds_read_b64_tr_b16 v[36:37], v255 offset:7296\n\t"
    "ds_read_b64_tr_b16 v[38:39], v255 offset:10880\n\t"
    "s_waitcnt lgkmcnt(15)\n\t"
    "v_mfma_f32_16x16x32_bf16 v[80:83], v[40:43], v[24:27], v[80:83]\n\t"
    "v_mfma_f32_16x16x32_bf16 v[84:87], v[40:43], v[28:31], v[84:87]\n\t"
    "ds_read_b64_tr_b16 v[40:41], v255 offset:32\n\t"
    "ds_read_b64_tr_b16 v[42:43], v255 offset:3616\n\t"
    "s_waitcnt lgkmcnt(15)\n\t"
    "v_mfma_f32_16x16x32_bf16 v[88:91], v[44:47], v[24:27], v[88:91]\n\t"
    "s_add_u32 m0, s16, 0x8000\n\t"
    "s_mov_b32 s4, 0x400000\n\t"
    "buffer_load_dwordx4 v199, s[52:55], s4 offen offset:1024 sc1 lds\n\t"
    "v_mfma_f32_16x16x32_bf16 v[92:95], v[44:47], v[28:31], v[92:95]\n\t"
    "ds_read_b64_tr_b16 v[44:45], v255 offset:7328\n\t"
    "ds_read_b64_tr_b16 v[46:47], v255 offset:10912\n\t"
    "s_waitcnt lgkmcnt(15)\n\t"
    "v_mfma_f32_16x16x32_bf16 v[96:99], v[48:51], v[24:27], v[96:99]\n\t"
    "v_mfma_f32_16x16x32_bf16 v[100:103], v[48:51], v[28:31], v[100:103]\n\t"
    "ds_read_b64_tr_b16 v[48:49], v255 offset:64\n\t"
    "ds_read_b64_tr_b16 v[50:51], v255 offset:3648\n\t"
    "s_waitcnt lgkmcnt(15)\n\t"
    "v_mfma_f32_16x16x32_bf16 v[104:107], v[52:55], v[24:27], v[104:107]\n\t"
    "v_mfma_f32_16x16x32_bf16 v[108:111], v[52:55], v[28:31], v[108:111]\n\t"
    "ds_read_b64_tr_b16 v[52:53], v255 offset:7360\n\t"
    "ds_read_b64_tr_b16 v[54:55], v255 offset:10944\n\t"
    "s_waitcnt lgkmcnt(15)\n\t"
    "v_mfma_f32_16x16x32_bf16 v[112:115], v[56:59], v[24:27], v[112:115]\n\t"
    "s_lshl_b32 s30, s13, 3\n\t"
    "s_add_u32 s30, s30, s8\n\t"
    "s_lshl_b32 s31, s12, 6\n\t"
    "s_add_u32 s31, s31, s30\n\t"
    "s_lshl_b32 s31, s31, 10\n\t"
    "buffer_load_dwordx4 v[192:195], v203, s[52:55], s31 offen sc1\n\t"
    "v_mfma_f32_16x16x32_bf16 v[116:119], v[56:59], v[28:31], v[116:119]\n\t"
    "ds_read_b64_tr_b16 v[56:57], v255 offset:96\n\t"
    "ds_read_b64_tr_b16 v[58:59], v255 offset:3680\n\t"
    "s_waitcnt lgkmcnt(15)\n\t"
    "v_mfma_f32_16x16x32_bf16 v[120:123], v[60:63], v[24:27], v[120:123]\n\t"
    "v_mfma_f32_16x16x32_bf16 v[124:127], v[60:63], v[28:31], v[124:127]\n\t"
    "ds_read_b64_tr_b16 v[60:61], v255 offset:7392\n\t"
    "ds_read_b64_tr_b16 v[62:63], v255 offset:10976\n\t"
    "s_waitcnt vmcnt(7)\n\t"
    "s_barrier\n\t"
    "s_waitcnt lgkmcnt(14)\n\t"
    "v_mfma_f32_16x16x32_bf16 v[64:67], v[32:35], v[216:219], v[64:67]\n\t"
    "ds_read_b128 v[24:27], v248\n\t"
    "ds_read_b128 v[28:31], v248 offset:2048\n\t"
    "v_mfma_f32_16x16x32_bf16 v[68:71], v[32:35], v[220:223], v[68:71]\n\t"
    "ds_read_b64_tr_b16 v[32:33], v255 offset:14592\n\t"
    "ds_read_b64_tr_b16 v[34:35], v255 offset:18176\n\t"
    "s_waitcnt lgkmcnt(15)\n\t"
    "v_mfma_f32_16x16x32_bf16 v[72:75], v[36:39], v[216:219], v[72:75]\n\t"
    "v_mfma_f32_16x16x32_bf16 v[76:79], v[36:39], v[220:223], v[76:79]\n\t"
    "ds_read_b64_tr_b16 v[36:37], v255 offset:21888\n\t"
    "ds_read_b64_tr_b16 v[38:39], v255 offset:25472\n\t"
    "s_waitcnt lgkmcnt(15)\n\t"
    "v_mfma_f32_16x16x32_bf16 v[80:83], v[40:43], v[216:219], v[80:83]\n\t"
    "v_mfma_f32_16x16x32_bf16 v[84:87], v[40:43], v[220:223], v[84:87]\n\t"
    "ds_read_b64_tr_b16 v[40:41], v255 offset:14624\n\t"
    "ds_read_b64_tr_b16 v[42:43], v255 offset:18208\n\t"
    "s_waitcnt lgkmcnt(15)\n\t"
    "v_mfma_f32_16x16x32_bf16 v[88:91], v[44:47], v[216:219], v[88:91]\n\t"
    "v_mfma_f32_16x16x32_bf16 v[92:95], v[44:47], v[220:223], v[92:95]\n\t"
    "ds_read_b64_tr_b16 v[44:45], v255 offset:21920\n\t"
    "ds_read_b64_tr_b16 v[46:47], v255 offset:25504\n\t"
    "s_waitcnt lgkmcnt(15)\n\t"
    "v_mfma_f32_16x16x32_bf16 v[96:99], v[48:51], v[216:219], v[96:99]\n\t"
    "v_mfma_f32_16x16x32_bf16 v[100:103], v[48:51], v[220:223], v[100:103]\n\t"
    "ds_read_b64_tr_b16 v[48:49], v255 offset:14656\n\t"
    "ds_read_b64_tr_b16 v[50:51], v255 offset:18240\n\t"
    "s_waitcnt lgkmcnt(15)\n\t"
    "v_mfma_f32_16x16x32_bf16 v[104:107], v[52:55], v[216:219], v[104:107]\n\t"
    "v_mfma_f32_16x16x32_bf16 v[108:111], v[52:55], v[220:223], v[108:111]\n\t"
    "ds_read_b64_tr_b16 v[52:53], v255 offset:21952\n\t"
    "ds_read_b64_tr_b16 v[54:55], v255 offset:25536\n\t"
    "s_waitcnt lgkmcnt(15)\n\t"
    "v_mfma_f32_16x16x32_bf16 v[112:115], v[56:59], v[216:219], v[112:115]\n\t"
    "v_mfma_f32_16x16x32_bf16 v[116:119], v[56:59], v[220:223], v[116:119]\n\t"
    "ds_read_b64_tr_b16 v[56:57], v255 offset:14688\n\t"
    "ds_read_b64_tr_b16 v[58:59], v255 offset:18272\n\t"
    "s_waitcnt lgkmcnt(15)\n\t"
    "v_mfma_f32_16x16x32_bf16 v[120:123], v[60:63], v[216:219], v[120:123]\n\t"
    "v_mfma_f32_16x16x32_bf16 v[124:127], v[60:63], v[220:223], v[124:127]\n\t"
    "ds_read_b64_tr_b16 v[60:61], v255 offset:21984\n\t"
    "ds_read_b64_tr_b16 v[62:63], v255 offset:25568\n\t"
    "s_waitcnt vmcnt(1)\n\t"
    "s_barrier\n\t"
    "ds_read_b128 v[176:179], v204\n\t"
    "s_mov_b64 exec, s[18:19]\n\t"
    "ds_read_b128 v[180:183], v205\n\t"
    "ds_read_b128 v[184:187], v205 offset:512\n\t"
    "s_mov_b64 exec, -1\n\t"
    "s_waitcnt lgkmcnt(15)\n\t"
    "v_mfma_f32_16x16x32_bf16 v[64:67], v[32:35], v[24:27], v[64:67]\n\t"
    "ds_read_b128 v[216:219], v249\n\t"
    "ds_read_b128 v[220:223], v249 offset:2048\n\t"
    "v_mfma_f32_16x16x32_bf16 v[68:71], v[32:35], v[28:31], v[68:71]\n\t"
    "ds_read_b64_tr_b16 v[32:33], v12\n\t"
    "ds_read_b64_tr_b16 v[34:35], v12 offset:3584\n\t"
    "v_mfma_f32_16x16x32_bf16 v[72:75], v[36:39], v[24:27], v[72:75]\n\t"
    "s_waitcnt vmcnt(0) lgkmcnt(6)\n\t"
    "v_mfma_f32_32x32x16_bf16 v[128:143], v[176:179], v[192:195], 0\n\t"
    "ds_read_b128 v[176:179], v204 offset:1024\n\t"
    "v_mfma_f32_16x16x32_bf16 v[76:79], v[36:39], v[28:31], v[76:79]\n\t"
    "ds_read_b64_tr_b16 v[36:37], v12 offset:7296\n\t"
    "ds_read_b64_tr_b16 v[38:39], v12 offset:10880\n\t"
    "v_mfma_f32_16x16x32_bf16 v[80:83], v[40:43], v[24:27], v[80:83]\n\t"
    "v_mfma_f32_16x16x32_bf16 v[84:87], v[40:43], v[28:31], v[84:87]\n\t"
    "ds_read_b64_tr_b16 v[40:41], v12 offset:32\n\t"
    "ds_read_b64_tr_b16 v[42:43], v12 offset:3616\n\t"
    "v_mfma_f32_16x16x32_bf16 v[88:91], v[44:47], v[24:27], v[88:91]\n\t"
    "v_mfma_f32_16x16x32_bf16 v[92:95], v[44:47], v[28:31], v[92:95]\n\t"
    "ds_read_b64_tr_b16 v[44:45], v12 offset:7328\n\t"
    "ds_read_b64_tr_b16 v[46:47], v12 offset:10912\n\t"
    "v_mfma_f32_16x16x32_bf16 v[96:99], v[48:51], v[24:27], v[96:99]\n\t"
    "v_mfma_f32_16x16x32_bf16 v[100:103], v[48:51], v[28:31], v[100:103]\n\t"
    "ds_read_b64_tr_b16 v[48:49], v12 offset:64\n\t"
    "ds_read_b64_tr_b16 v[50:51], v12 offset:3648\n\t"
    "v_max3_f32 v210, v128, v129, v130\n\t"
    "v_max3_f32 v211, v131, v132, v133\n\t"
    "v_mfma_f32_16x16x32_bf16 v[104:107], v[52:55], v[24:27], v[104:107]\n\t"
    "v_max3_f32 v210, v210, v134, v135\n\t"
    "v_max3_f32 v211, v211, v136, v137\n\t"
    "v_mfma_f32_16x16x32_bf16 v[108:111], v[52:55], v[28:31], v[108:111]\n\t"
    "ds_read_b64_tr_b16 v[52:53], v12 offset:7360\n\t"
    "ds_read_b64_tr_b16 v[54:55], v12 offset:10944\n\t"
    "v_max3_f32 v210, v210, v138, v139\n\t"
    "v_max3_f32 v211, v211, v140, v141\n\t"
    "v_mfma_f32_16x16x32_bf16 v[112:115], v[56:59], v[24:27], v[112:115]\n\t"
    "v_max3_f32 v210, v210, v142, v143\n\t"
    "v_max_f32 v210, v210, v211\n\t"
    "v_mfma_f32_16x16x32_bf16 v[116:119], v[56:59], v[28:31], v[116:119]\n\t"
    "ds_read_b64_tr_b16 v[56:57], v12 offset:96\n\t"
    "ds_read_b64_tr_b16 v[58:59], v12 offset:3680\n\t"
    "v_mov_b32 v211, v210\n\t"
    "s_nop 1\n\t"
    "v_permlane32_swap_b32 v210, v211\n\t"
    "v_mfma_f32_16x16x32_bf16 v[120:123], v[60:63], v[24:27], v[120:123]\n\t"
    "v_max_f32 v210, v210, v211\n\t"
    "v_sub_f32 v144, 0, v210\n\t"
    "v_mfma_f32_16x16x32_bf16 v[124:127], v[60:63], v[28:31], v[124:127]\n\t"
    "ds_read_b64_tr_b16 v[60:61], v12 offset:7392\n\t"
    "ds_read_b64_tr_b16 v[62:63], v12 offset:10976\n\t"
    "s_barrier\n\t"
    "v_sub_f32 v145, 0, v210\n\t"
    "v_sub_f32 v146, 0, v210\n\t"
    "s_waitcnt lgkmcnt(15)\n\t"
    "v_mfma_f32_16x16x32_bf16 v[64:67], v[32:35], v[216:219], v[64:67]\n\t"
    "v_sub_f32 v147, 0, v210\n\t"
    "v_sub_f32 v148, 0, v210\n\t"
    "v_mfma_f32_16x16x32_bf16 v[68:71], v[32:35], v[220:223], v[68:71]\n\t"
    "v_sub_f32 v149, 0, v210\n\t"
    "v_sub_f32 v150, 0, v210\n\t"
    "s_waitcnt lgkmcnt(12)\n\t"
    "v_mfma_f32_16x16x32_bf16 v[72:75], v[36:39], v[216:219], v[72:75]\n\t"
    "v_sub_f32 v151, 0, v210\n\t"
    "v_sub_f32 v152, 0, v210\n\t"
    "v_mfma_f32_16x16x32_bf16 v[76:79], v[36:39], v[220:223], v[76:79]\n\t"
    "v_sub_f32 v153, 0, v210\n\t"
    "v_sub_f32 v154, 0, v210\n\t"
    "s_waitcnt lgkmcnt(10)\n\t"
    "v_mfma_f32_16x16x32_bf16 v[80:83], v[40:43], v[216:219], v[80:83]\n\t"
    "v_sub_f32 v155, 0, v210\n\t"
    "v_sub_f32 v156, 0, v210\n\t"
    "v_mfma_f32_16x16x32_bf16 v[84:87], v[40:43], v[220:223], v[84:87]\n\t"
    "v_sub_f32 v157, 0, v210\n\t"
    "v_sub_f32 v158, 0, v210\n\t"
    "s_waitcnt lgkmcnt(8)\n\t"
    "v_mfma_f32_16x16x32_bf16 v[88:91], v[44:47], v[216:219], v[88:91]\n\t"
    "v_sub_f32 v159, 0, v210\n\t"
    "v_sub_f32 v128, v128, v210\n\t"
    "v_mfma_f32_16x16x32_bf16 v[92:95], v[44:47], v[220:223], v[92:95]\n\t"
    "v_sub_f32 v129, v129, v210\n\t"
    "v_sub_f32 v130, v130, v210\n\t"
    "s_waitcnt lgkmcnt(6)\n\t"
    "v_mfma_f32_16x16x32_bf16 v[96:99], v[48:51], v[216:219], v[96:99]\n\t"
    "v_sub_f32 v131, v131, v210\n\t"
    "v_sub_f32 v132, v132, v210\n\t"
    "v_mfma_f32_16x16x32_bf16 v[100:103], v[48:51], v[220:223], v[100:103]\n\t"
    "v_sub_f32 v133, v133, v210\n\t"
    "v_sub_f32 v134, v134, v210\n\t"
    "s_waitcnt lgkmcnt(4)\n\t"
    "v_mfma_f32_16x16x32_bf16 v[104:107], v[52:55], v[216:219], v[104:107]\n\t"
    "v_sub_f32 v135, v135, v210\n\t"
    "v_sub_f32 v136, v136, v210\n\t"
    "v_mfma_f32_16x16x32_bf16 v[108:111], v[52:55], v[220:223], v[108:111]\n\t"
    "v_sub_f32 v137, v137, v210\n\t"
    "v_sub_f32 v138, v138, v210\n\t"
    "s_waitcnt lgkmcnt(2)\n\t"
    "v_mfma_f32_16x16x32_bf16 v[112:115], v[56:59], v[216:219], v[112:115]\n\t"
    "v_sub_f32 v139, v139, v210\n\t"
    "v_sub_f32 v140, v140, v210\n\t"
    "v_mfma_f32_16x16x32_bf16 v[116:119], v[56:59], v[220:223], v[116:119]\n\t"
    "v_sub_f32 v141, v141, v210\n\t"
    "v_sub_f32 v142, v142, v210\n\t"
    "s_waitcnt lgkmcnt(0)\n\t"
    "v_mfma_f32_16x16x32_bf16 v[120:123], v[60:63], v[216:219], v[120:123]\n\t"
    "v_sub_f32 v143, v143, v210\n\t"
    "v_mfma_f32_16x16x32_bf16 v[124:127], v[60:63], v[220:223], v[124:127]\n\t"
    "s_barrier\n\t"
    "s_setprio 2\n\t"
    "v_exp_f32 v128, v128\n\t"
    "v_exp_f32 v129, v129\n\t"
    "v_exp_f32 v130, v130\n\t"
    "v_exp_f32 v131, v131\n\t"
    "v_exp_f32 v132, v132\n\t"
    "v_mfma_f32_32x32x16_bf16 v[32:47], v[176:179], v[192:195], v[144:159]\n\t"
    "ds_read_b128 v[176:179], v204 offset:2048\n\t"
    "v_exp_f32 v133, v133\n\t"
    "v_mul_f32 v64, v64, v72\n\t"
    "v_mul_f32 v65, v65, v73\n\t"
    "v_mul_f32 v66, v66, v74\n\t"
    "v_mul_f32 v67, v67, v75\n\t"
    "v_mul_f32 v68, v68, v76\n\t"
    "v_mul_f32 v69, v69, v77\n\t"
    "v_mul_f32 v70, v70, v78\n\t"
    "v_mul_f32 v71, v71, v79\n\t"
    "v_cvt_pk_bf16_f32 v232, v64, v65\n\t"
    "v_cvt_pk_bf16_f32 v233, v66, v67\n\t"
    "v_cvt_pk_bf16_f32 v234, v68, v69\n\t"
    "v_cvt_pk_bf16_f32 v235, v70, v71\n\t"
    "s_nop 0\n\t"
    "v_permlane16_swap_b32 v232, v234\n\t"
    "v_permlane16_swap_b32 v233, v235\n\t"
    "buffer_store_dwordx4 v[232:235], v18, s[52:55], 0 offen sc1\n\t"
    "v_exp_f32 v134, v134\n\t"
    "v_exp_f32 v135, v135\n\t"
    "v_exp_f32 v136, v136\n\t"
    "v_mul_f32 v80, v80, v88\n\t"
    "v_mul_f32 v81, v81, v89\n\t"
    "v_mul_f32 v82, v82, v90\n\t"
    "v_mul_f32 v83, v83, v91\n\t"
    "v_mul_f32 v84, v84, v92\n\t"
    "v_mul_f32 v85, v85, v93\n\t"
    "v_mul_f32 v86, v86, v94\n\t"
    "v_mul_f32 v87, v87, v95\n\t"
    "v_cvt_pk_bf16_f32 v236, v80, v81\n\t"
    "v_cvt_pk_bf16_f32 v237, v82, v83\n\t"
    "v_cvt_pk_bf16_f32 v238, v84, v85\n\t"
    "v_cvt_pk_bf16_f32 v239, v86, v87\n\t"
    "s_nop 0\n\t"
    "v_permlane16_swap_b32 v236, v238\n\t"
    "v_permlane16_swap_b32 v237, v239\n\t"
    "buffer_store_dwordx4 v[236:239], v18, s[52:55], 0 offen offset:1024 sc1\n\t"
    "v_exp_f32 v137, v137\n\t"
    "v_smfmac_f32_32x32x32_bf16 v[160:175], v[180:183], v[128:135], v206\n\t"
    "s_mov_b64 exec, s[18:19]\n\t"
    "ds_read_b128 v[180:183], v205 offset:1024\n\t"
    "ds_read_b128 v[188:191], v205 offset:1536\n\t"
    "s_mov_b64 exec, -1\n\t"
    "v_exp_f32 v138, v138\n\t"
    "v_exp_f32 v139, v139\n\t"
    "v_mul_f32 v96, v96, v104\n\t"
    "v_mul_f32 v97, v97, v105\n\t"
    "v_mul_f32 v98, v98, v106\n\t"
    "v_mul_f32 v99, v99, v107\n\t"
    "v_mul_f32 v100, v100, v108\n\t"
    "v_mul_f32 v101, v101, v109\n\t"
    "v_mul_f32 v102, v102, v110\n\t"
    "v_mul_f32 v103, v103, v111\n\t"
    "v_cvt_pk_bf16_f32 v240, v96, v97\n\t"
    "v_cvt_pk_bf16_f32 v241, v98, v99\n\t"
    "v_cvt_pk_bf16_f32 v242, v100, v101\n\t"
    "v_cvt_pk_bf16_f32 v243, v102, v103\n\t"
    "s_nop 0\n\t"
    "v_permlane16_swap_b32 v240, v242\n\t"
    "v_permlane16_swap_b32 v241, v243\n\t"
    "buffer_store_dwordx4 v[240:243], v18, s[52:55], 0 offen offset:2048 sc1\n\t"
    "v_exp_f32 v140, v140\n\t"
    "v_exp_f32 v141, v141\n\t"
    "v_exp_f32 v142, v142\n\t"
    "v_mul_f32 v112, v112, v120\n\t"
    "v_mul_f32 v113, v113, v121\n\t"
    "v_mul_f32 v114, v114, v122\n\t"
    "v_mul_f32 v115, v115, v123\n\t"
    "v_mul_f32 v116, v116, v124\n\t"
    "v_mul_f32 v117, v117, v125\n\t"
    "v_mul_f32 v118, v118, v126\n\t"
    "v_mul_f32 v119, v119, v127\n\t"
    "v_cvt_pk_bf16_f32 v244, v112, v113\n\t"
    "v_cvt_pk_bf16_f32 v245, v114, v115\n\t"
    "v_cvt_pk_bf16_f32 v246, v116, v117\n\t"
    "v_cvt_pk_bf16_f32 v247, v118, v119\n\t"
    "s_nop 0\n\t"
    "v_permlane16_swap_b32 v244, v246\n\t"
    "v_permlane16_swap_b32 v245, v247\n\t"
    "buffer_store_dwordx4 v[244:247], v18, s[52:55], 0 offen offset:3072 sc1\n\t"
    "v_exp_f32 v143, v143\n\t"
    "s_branch L_join_%=\n\t"
    "L_s1_retry_%=:\n\t"
    "s_add_u32 s84, s84, 1\n\t"
    "s_cmp_gt_u32 s84, 0x20000\n\t"
    "s_cbranch_scc1 L_s1_ok_%=\n\t"
    "s_sleep 2\n\t"
    "buffer_load_dwordx2 v[200:201], v202, s[52:55], 0 offen sc1\n\t"
    "s_waitcnt vmcnt(0)\n\t"
    "v_cmp_ne_u64 vcc, s[82:83], v[200:201]\n\t"
    "s_nop 4\n\t"
    "s_cbranch_vccnz L_s1_retry_%=\n\t"
    "s_branch L_s1_ok_%=\n\t"
    "L_s2_retry_%=:\n\t"
    "s_add_u32 s84, s84, 1\n\t"
    "s_cmp_gt_u32 s84, 0x20000\n\t"
    "s_cbranch_scc1 L_s2_ok_%=\n\t"
    "s_sleep 4\n\t"
    "buffer_load_dwordx2 v[200:201], v202, s[52:55], 0 offen sc1\n\t"
    "s_waitcnt vmcnt(0)\n\t"
    "v_cmp_ne_u64 vcc, s[82:83], v[200:201]\n\t"
    "s_nop 4\n\t"
    "s_cbranch_vccnz L_s2_retry_%=\n\t"
    "s_branch L_s2_ok_%=\n\t"
    "L_sa_retry_%=:\n\t"
    "s_add_u32 s84, s84, 1\n\t"
    "s_cmp_gt_u32 s84, 0x20000\n\t"
    "s_cbranch_scc1 L_sa_ok_%=\n\t"
    "s_sleep 4\n\t"
    "buffer_load_dwordx2 v[200:201], v202, s[52:55], 0 offen sc1\n\t"
    "s_waitcnt vmcnt(0)\n\t"
    "v_cmp_ne_u64 vcc, s[82:83], v[200:201]\n\t"
    "s_nop 4\n\t"
    "s_cbranch_vccnz L_sa_retry_%=\n\t"
    "s_branch L_sa_ok_%=\n\t"
    "L_fb_%=:\n\t"
    "v_add_u32 v208, 0xfffe8000, v205\n\t"
    "v_mov_b32 v210, 0xff800000\n\t"
    "s_lshl_b32 s30, s12, 16\n\t"
    "s_add_u32 s11, s30, 0x200000\n\t"
    "s_mov_b32 s26, 0\n\t"
    "L_fb1_%=:\n\t"
    "buffer_load_dwordx4 v[176:179], v203, s[52:55], s11 offen sc1\n\t"
    "s_waitcnt vmcnt(0)\n\t"
    "v_mfma_f32_32x32x16_bf16 v[128:143], v[176:179], v[192:195], 0\n\t"
    "s_nop 15\n\t"
    "v_max3_f32 v210, v210, v128, v129\n\t"
    "v_max3_f32 v210, v210, v130, v131\n\t"
    "v_max3_f32 v210, v210, v132, v133\n\t"
    "v_max3_f32 v210, v210, v134, v135\n\t"
    "v_max3_f32 v210, v210, v136, v137\n\t"
    "v_max3_f32 v210, v210, v138, v139\n\t"
    "v_max3_f32 v210, v210, v140, v141\n\t"
    "v_max3_f32 v210, v210, v142, v143\n\t"
    "s_add_u32 s11, s11, 0x400\n\t"
    "s_add_u32 s26, s26, 1\n\t"
    "s_cmp_lt_u32 s26, 64\n\t"
    "s_cbranch_scc1 L_fb1_%=\n\t"
    "v_mov_b32 v211, v210\n\t"
    "s_nop 1\n\t"
    "v_permlane32_swap_b32 v210, v211\n\t"
    "s_nop 1\n\t"
    "v_max_f32 v210, v210, v211\n\t"
    "v_sub_f32 v144, 0, v210\n\t"
    "v_sub_f32 v145, 0, v210\n\t"
    "v_sub_f32 v146, 0, v210\n\t"
    "v_sub_f32 v147, 0, v210\n\t"
    "v_sub_f32 v148, 0, v210\n\t"
    "v_sub_f32 v149, 0, v210\n\t"
    "v_sub_f32 v150, 0, v210\n\t"
    "v_sub_f32 v151, 0, v210\n\t"
    "v_sub_f32 v152, 0, v210\n\t"
    "v_sub_f32 v153, 0, v210\n\t"
    "v_sub_f32 v154, 0, v210\n\t"
    "v_sub_f32 v155, 0, v210\n\t"
    "v_sub_f32 v156, 0, v210\n\t"
    "v_sub_f32 v157, 0, v210\n\t"
    "v_sub_f32 v158, 0, v210\n\t"
    "v_sub_f32 v159, 0, v210\n\t"
    "v_mov_b32 v160, 0\n\t"
    "v_mov_b32 v161, 0\n\t"
    "v_mov_b32 v162, 0\n\t"
    "v_mov_b32 v163, 0\n\t"
    "v_mov_b32 v164, 0\n\t"
    "v_mov_b32 v165, 0\n\t"
    "v_mov_b32 v166, 0\n\t"
    "v_mov_b32 v167, 0\n\t"
    "v_mov_b32 v168, 0\n\t"
    "v_mov_b32 v169, 0\n\t"
    "v_mov_b32 v170, 0\n\t"
    "v_mov_b32 v171, 0\n\t"
    "v_mov_b32 v172, 0\n\t"
    "v_mov_b32 v173, 0\n\t"
    "v_mov_b32 v174, 0\n\t"
    "v_mov_b32 v175, 0\n\t"
    "s_add_u32 s11, s30, 0x200000\n\t"
    "s_add_u32 s30, s30, 0x400000\n\t"
    "s_mov_b32 s26, 0\n\t"
    "L_fb2_%=:\n\t"
    "buffer_load_dwordx4 v[176:179], v203, s[52:55], s11 offen sc1\n\t"
    "s_mov_b64 exec, s[18:19]\n\t"
    "buffer_load_dwordx4 v[180:183], v208, s[52:55], s30 offen sc1\n\t"
    "buffer_load_dwordx4 v[184:187], v208, s[52:55], s30 offen offset:512 sc1\n\t"
    "s_mov_b64 exec, -1\n\t"
    "s_waitcnt vmcnt(0)\n\t"
    "v_mfma_f32_32x32x16_bf16 v[128:143], v[176:179], v[192:195], v[144:159]\n\t"
    "s_nop 15\n\t"
    "v_exp_f32 v128, v128\n\t"
    "v_exp_f32 v129, v129\n\t"
    "v_exp_f32 v130, v130\n\t"
    "v_exp_f32 v131, v131\n\t"
    "v_exp_f32 v132, v132\n\t"
    "v_exp_f32 v133, v133\n\t"
    "v_exp_f32 v134, v134\n\t"
    "v_exp_f32 v135, v135\n\t"
    "v_exp_f32 v136, v136\n\t"
    "v_exp_f32 v137, v137\n\t"
    "v_exp_f32 v138, v138\n\t"
    "v_exp_f32 v139, v139\n\t"
    "v_exp_f32 v140, v140\n\t"
    "v_exp_f32 v141, v141\n\t"
    "v_exp_f32 v142, v142\n\t"
    "v_exp_f32 v143, v143\n\t"
    "s_nop 2\n\t"
    "v_smfmac_f32_32x32x32_bf16 v[160:175], v[180:183], v[128:135], v206\n\t"
    "v_smfmac_f32_32x32x32_bf16 v[160:175], v[184:187], v[136:143], v206\n\t"
    "s_nop 15\n\t"
    "s_add_u32 s11, s11, 0x400\n\t"
    "s_add_u32 s30, s30, 0x400\n\t"
    "s_add_u32 s26, s26, 1\n\t"
    "s_cmp_lt_u32 s26, 64\n\t"
    "s_cbranch_scc1 L_fb2_%=\n\t"
    "s_nop 15\n\t"
    "s_branch L_fb_done_%=\n\t"
    "L_o0_retry_%=:\n\t"
    "s_add_u32 s84, s84, 1\n\t"
    "s_cmp_gt_u32 s84, 0x20000\n\t"
    "s_cbranch_scc1 L_o0_ok_%=\n\t"
    "s_sleep 2\n\t"
    "s_waitcnt vmcnt(0)\n\t"
    "s_mov_b32 s11, 0x1c000\n\t"
    "buffer_load_dwordx4 v[216:219], v20, s[52:55], s11 offen sc1\n\t"
    "s_mov_b32 s11, 0x40000\n\t"
    "buffer_load_dwordx4 v[220:223], v20, s[52:55], s11 offen sc1\n\t"
    "s_mov_b32 s11, 0x1e000\n\t"
    "buffer_load_dwordx4 v[224:227], v20, s[52:55], s11 offen sc1\n\t"
    "s_mov_b32 s11, 0x42000\n\t"
    "buffer_load_dwordx4 v[228:231], v20, s[52:55], s11 offen sc1\n\t"
    "s_mov_b32 s11, 0x20000\n\t"
    "buffer_load_dwordx4 v[232:235], v20, s[52:55], s11 offen sc1\n\t"
    "s_mov_b32 s11, 0x44000\n\t"
    "buffer_load_dwordx4 v[236:239], v20, s[52:55], s11 offen sc1\n\t"
    "s_mov_b32 s11, 0x22000\n\t"
    "buffer_load_dwordx4 v[240:243], v20, s[52:55], s11 offen sc1\n\t"
    "s_mov_b32 s11, 0x46000\n\t"
    "buffer_load_dwordx4 v[244:247], v20, s[52:55], s11 offen sc1\n\t"
    "s_waitcnt vmcnt(0)\n\t"
    "v_max3_u32 v208, v216, v217, v218\n\t"
    "v_max3_u32 v209, v219, v220, v221\n\t"
    "v_max3_u32 v208, v208, v222, v223\n\t"
    "v_max_u32 v208, v208, v209\n\t"
    "v_cmp_eq_u32 vcc, -1, v208\n\t"
    "s_nop 4\n\t"
    "s_cbranch_vccnz L_o0_retry_%=\n\t"
    "s_branch L_o0_ok_%=\n\t"
    "L_o1_retry_%=:\n\t"
    "s_add_u32 s84, s84, 1\n\t"
    "s_cmp_gt_u32 s84, 0x20000\n\t"
    "s_cbranch_scc1 L_o1_ok_%=\n\t"
    "s_sleep 2\n\t"
    "s_waitcnt vmcnt(0)\n\t"
    "s_mov_b32 s11, 0x1e000\n\t"
    "buffer_load_dwordx4 v[224:227], v20, s[52:55], s11 offen sc1\n\t"
    "s_mov_b32 s11, 0x42000\n\t"
    "buffer_load_dwordx4 v[228:231], v20, s[52:55], s11 offen sc1\n\t"
    "s_mov_b32 s11, 0x20000\n\t"
    "buffer_load_dwordx4 v[232:235], v20, s[52:55], s11 offen sc1\n\t"
    "s_mov_b32 s11, 0x44000\n\t"
    "buffer_load_dwordx4 v[236:239], v20, s[52:55], s11 offen sc1\n\t"
    "s_mov_b32 s11, 0x22000\n\t"
    "buffer_load_dwordx4 v[240:243], v20, s[52:55], s11 offen sc1\n\t"
    "s_mov_b32 s11, 0x46000\n\t"
    "buffer_load_dwordx4 v[244:247], v20, s[52:55], s11 offen sc1\n\t"
    "s_waitcnt vmcnt(0)\n\t"
    "v_max3_u32 v208, v224, v225, v226\n\t"
    "v_max3_u32 v209, v227, v228, v229\n\t"
    "v_max3_u32 v208, v208, v230, v231\n\t"
    "v_max_u32 v208, v208, v209\n\t"
    "v_cmp_eq_u32 vcc, -1, v208\n\t"
    "s_nop 4\n\t"
    "s_cbranch_vccnz L_o1_retry_%=\n\t"
    "s_branch L_o1_ok_%=\n\t"
    "L_o2_retry_%=:\n\t"
    "s_add_u32 s84, s84, 1\n\t"
    "s_cmp_gt_u32 s84, 0x20000\n\t"
    "s_cbranch_scc1 L_o2_ok_%=\n\t"
    "s_sleep 2\n\t"
    "s_waitcnt vmcnt(0)\n\t"
    "s_mov_b32 s11, 0x20000\n\t"
    "buffer_load_dwordx4 v[232:235], v20, s[52:55], s11 offen sc1\n\t"
    "s_mov_b32 s11, 0x44000\n\t"
    "buffer_load_dwordx4 v[236:239], v20, s[52:55], s11 offen sc1\n\t"
    "s_mov_b32 s11, 0x22000\n\t"
    "buffer_load_dwordx4 v[240:243], v20, s[52:55], s11 offen sc1\n\t"
    "s_mov_b32 s11, 0x46000\n\t"
    "buffer_load_dwordx4 v[244:247], v20, s[52:55], s11 offen sc1\n\t"
    "s_waitcnt vmcnt(0)\n\t"
    "v_max3_u32 v208, v232, v233, v234\n\t"
    "v_max3_u32 v209, v235, v236, v237\n\t"
    "v_max3_u32 v208, v208, v238, v239\n\t"
    "v_max_u32 v208, v208, v209\n\t"
    "v_cmp_eq_u32 vcc, -1, v208\n\t"
    "s_nop 4\n\t"
    "s_cbranch_vccnz L_o2_retry_%=\n\t"
    "s_branch L_o2_ok_%=\n\t"
    "L_o3_retry_%=:\n\t"
    "s_add_u32 s84, s84, 1\n\t"
    "s_cmp_gt_u32 s84, 0x20000\n\t"
    "s_cbranch_scc1 L_o3_ok_%=\n\t"
    "s_sleep 2\n\t"
    "s_waitcnt vmcnt(0)\n\t"
    "s_mov_b32 s11, 0x22000\n\t"
    "buffer_load_dwordx4 v[240:243], v20, s[52:55], s11 offen sc1\n\t"
    "s_mov_b32 s11, 0x46000\n\t"
    "buffer_load_dwordx4 v[244:247], v20, s[52:55], s11 offen sc1\n\t"
    "s_waitcnt vmcnt(0)\n\t"
    "v_max3_u32 v208, v240, v241, v242\n\t"
    "v_max3_u32 v209, v243, v244, v245\n\t"
    "v_max3_u32 v208, v208, v246, v247\n\t"
    "v_max_u32 v208, v208, v209\n\t"
    "v_cmp_eq_u32 vcc, -1, v208\n\t"
    "s_nop 4\n\t"
    "s_cbranch_vccnz L_o3_retry_%=\n\t"
    "s_branch L_o3_ok_%=\n\t"
    "L_end_%=:\n\t"
    :: "s"(__builtin_amdgcn_kernarg_segment_ptr()), "s"(blockIdx.x), "v"(threadIdx.x), "s"((unsigned)(size_t)lds)
    : "v1", "v2", "v3", "v4", "v5", "v6", "v7", "v8", "v9", "v10", "v11", "v12", "v13", "v14", "v15", "v16", "v17", "v18", "v19", "v20", "v21", "v22", "v23", "v24", "v25", "v26", "v27", "v28", "v29", "v30", "v31", "v32", "v33", "v34", "v35", "v36", "v37", "v38", "v39", "v40", "v41", "v42", "v43", "v44", "v45", "v46", "v47", "v48", "v49", "v50", "v51", "v52", "v53", "v54", "v55", "v56", "v57", "v58", "v59", "v60", "v61", "v62", "v63", "v64", "v65", "v66", "v67", "v68", "v69", "v70", "v71", "v72", "v73", "v74", "v75", "v76", "v77", "v78", "v79", "v80", "v81", "v82", "v83", "v84", "v85", "v86", "v87", "v88", "v89", "v90", "v91", "v92", "v93", "v94", "v95", "v96", "v97", "v98", "v99", "v100", "v101", "v102", "v103", "v104", "v105", "v106", "v107", "v108", "v109", "v110", "v111", "v112", "v113", "v114", "v115", "v116", "v117", "v118", "v119", "v120", "v121", "v122", "v123", "v124", "v125", "v126", "v127", "v128", "v129", "v130", "v131", "v132", "v133", "v134", "v135", "v136", "v137", "v138", "v139", "v140", "v141", "v142", "v143", "v144", "v145", "v146", "v147", "v148", "v149", "v150", "v151", "v152", "v153", "v154", "v155", "v156", "v157", "v158", "v159", "v160", "v161", "v162", "v163", "v164", "v165", "v166", "v167", "v168", "v169", "v170", "v171", "v172", "v173", "v174", "v175", "v176", "v177", "v178", "v179", "v180", "v181", "v182", "v183", "v184", "v185", "v186", "v187", "v188", "v189", "v190", "v191", "v192", "v193", "v194", "v195", "v196", "v197", "v198", "v199", "v200", "v201", "v202", "v203", "v204", "v205", "v206", "v207", "v208", "v209", "v210", "v211", "v212", "v213", "v214", "v215", "v216", "v217", "v218", "v219", "v220", "v221", "v222", "v223", "v224", "v225", "v226", "v227", "v228", "v229", "v230", "v231", "v232", "v233", "v234", "v235", "v236", "v237", "v238", "v239", "v240", "v241", "v242", "v243", "v244", "v245", "v246", "v247", "v248", "v249", "v250", "v251", "v252", "v253", "v254", "v255", "s4", "s5", "s6", "s7", "s8", "s9", "s10", "s11", "s12", "s13", "s14", "s15", "s16", "s17", "s18", "s19", "s20", "s21", "s22", "s23", "s24", "s25", "s26", "s27", "s28", "s29", "s30", "s31", "s33", "s34", "s35", "s36", "s37", "s38", "s39", "s40", "s41", "s42", "s43", "s44", "s45", "s46", "s47", "s48", "s49", "s50", "s51", "s52", "s53", "s54", "s55", "s56", "s57", "s58", "s59", "s60", "s61", "s62", "s63", "s64", "s65", "s66", "s67", "s68", "s69", "s70", "s71", "s72", "s73", "s74", "s75", "s76", "s77", "s78", "s79", "s80", "s81", "s82", "s83", "s84", "s85", "s86", "s87", "s88", "s89", "s90", "s91", "s92", "s93", "s94", "s95", "s96", "s97", "s98", "s99", "vcc", "memory");
}

extern "C" void kernel_launch(void* const* d_in, const int* in_sizes, int n_in, void* d_out, int out_size, void* d_ws, size_t ws_size, hipStream_t stream) {
  gated_mix__softmax_full_mha__glu_gated_bilinear__prompt_kernel<<<256, 512, 0, stream>>>(d_in[0], d_in[1], d_in[2], d_in[4], d_in[5], d_in[6], d_in[7], d_out, d_ws);
}
```
